```python
import torch

def _softmax(x, axis=-1):
    return torch.softmax(x, dim=axis)

def _gelu(x):
    return torch.nn.functional.gelu(x, approximate='tanh')

def _silu(x):
    return torch.nn.functional.silu(x)

def _sigmoid(x):
    return torch.sigmoid(x)

def _layernorm(x, axis=-1, eps=1e-05):
    m = x.mean(dim=axis, keepdim=True)
    v = x.var(dim=axis, keepdim=True, unbiased=False)
    return (x - m) / torch.sqrt(v + eps)

def _scatter_add(tgt, idx, src, axis=0):
    return tgt.clone().index_add_(axis, idx, src)

def _segment_sum(src, seg, n):
    out = torch.zeros((n,) + src.shape[1:], dtype=src.dtype)
    return out.index_add_(0, seg, src)

def _pad(x, pw):
    flat = tuple((v for pair in reversed(pw) for v in pair))
    return torch.nn.functional.pad(x, flat)
kernel_name = 'parallel_sum__memory_aug_recurrent_mem_tr__geglu_ffn__prompt'

def setup_inputs():

    _w = lambda a: (lambda t: t.to(torch.bfloat16) if t.is_floating_point() else t)(torch.as_tensor(a))

    def _A_setup():
        B, S, Dm = (1, 2048, 576)
        H, Dh = (32, 128)
        M, seg = (8, 32)
        seg = min(seg, S)
        S = S // seg * seg
        return {'x': _w((lambda *s: torch.randn(*s, dtype=torch.bfloat16) * 0.05)(B, S, Dm)), 'mem0': _w((lambda *s: torch.randn(*s, dtype=torch.bfloat16) * 1.6)(B, M, Dm)), 'Wq': _w((lambda *s: torch.randn(*s, dtype=torch.bfloat16) * 0.8)(Dm, H, Dh) * Dm ** (-0.5)), 'Wk': _w((lambda *s: torch.randn(*s, dtype=torch.bfloat16) * 0.8)(Dm, H, Dh) * Dm ** (-0.5)), 'Wv': _w((lambda *s: torch.randn(*s, dtype=torch.bfloat16) * 1.6)(Dm, H, Dh) * Dm ** (-0.5)), 'Wo': _w((lambda *s: torch.randn(*s, dtype=torch.bfloat16) * 1.6)(H, Dh, Dm) * (H * Dh) ** (-0.5)), 'seg_len': seg}
    _A_in = _A_setup()

    def _B_setup():
        B, S, D, F = (1, 2048, 576, 4608)
        return {'x': _w((lambda *s: torch.randn(*s, dtype=torch.bfloat16) * 0.05)(B, S, D)), 'w_gate': _w((lambda *s: torch.randn(*s, dtype=torch.bfloat16) * 0.05)(D, F)), 'w_up': _w((lambda *s: torch.randn(*s, dtype=torch.bfloat16) * 0.05)(D, F)), 'w_down': _w((lambda *s: torch.randn(*s, dtype=torch.bfloat16) * 0.05)(F, D))}
    _B_in = _B_setup()
    return {'A_x': _A_in['x'], 'A_mem0': _A_in['mem0'], 'A_Wq': _A_in['Wq'], 'A_Wk': _A_in['Wk'], 'A_Wv': _A_in['Wv'], 'A_Wo': _A_in['Wo'], 'A_seg_len': _A_in['seg_len'], 'B_x': _B_in['x'], 'B_w_gate': _B_in['w_gate'], 'B_w_up': _B_in['w_up'], 'B_w_down': _B_in['w_down']}

def reference(A_x, A_mem0, A_Wq, A_Wk, A_Wv, A_Wo, A_seg_len, B_x, B_w_gate, B_w_up, B_w_down):

    def _A_ref(x, mem0, Wq, Wk, Wv, Wo, seg_len):
        B, S, Dm = x.shape
        H, Dh = (Wq.shape[1], Wq.shape[2])
        M = mem0.shape[1]
        scale = Dh ** (-0.5)
        n_seg = S // seg_len
        L = M + seg_len
        idx = torch.arange(L)
        mask = (idx[:, None] < M) | (idx[:, None] >= idx[None, :])
        mem = mem0
        outs = []
        for s in range(n_seg):
            xs = x[:, s * seg_len:(s + 1) * seg_len, :].clone()
            h = torch.concatenate([mem, xs], axis=1)
            q = torch.einsum('btd,dhe->bhte', h, Wq)
            k = torch.einsum('btd,dhe->bhte', h, Wk)
            v = torch.einsum('btd,dhe->bhte', h, Wv)
            scores = torch.einsum('bhqe,bhke->bhqk', q, k) * scale
            scores = torch.where(mask[None, None], scores, -1000000000.0)
            p = _softmax(scores, axis=-1)
            o = torch.einsum('bhqk,bhke->bhqe', p, v)
            o = torch.einsum('bhte,hed->btd', o, Wo)
            mem = o[:, :M, :].clone()
            outs.append(o[:, M:, :])
        return torch.concatenate(outs, axis=1)

    def _B_ref(x, w_gate, w_up, w_down):
        return _gelu(x @ w_gate) * (x @ w_up) @ w_down
    x = A_x
    xn = _layernorm(x, axis=-1)
    return x + _A_ref(xn, A_mem0, A_Wq, A_Wk, A_Wv, A_Wo, A_seg_len) + _B_ref(xn, B_w_gate, B_w_up, B_w_down)
```

```cpp
#include <hip/hip_runtime.h>
#include <hip/hip_bf16.h>
#include <stdint.h>

typedef __bf16 bf8 __attribute__((ext_vector_type(8)));
typedef __bf16 bf16x16 __attribute__((ext_vector_type(16)));
typedef __bf16 bfx2 __attribute__((ext_vector_type(2)));
typedef short s4v __attribute__((ext_vector_type(4)));
typedef float f4 __attribute__((ext_vector_type(4)));
typedef float f2 __attribute__((ext_vector_type(2)));
typedef unsigned u4 __attribute__((ext_vector_type(4)));
typedef unsigned u2 __attribute__((ext_vector_type(2)));
typedef unsigned u8v __attribute__((ext_vector_type(8)));
#define LAS __attribute__((address_space(3)))
#define GAS __attribute__((address_space(1)))
#define DEV __device__ __forceinline__

constexpr int S_TOK = 2048, DM = 576, NHEAD = 32, DH = 128, NSEG = 64, FF = 4608;
constexpr int N_QK = 32, N_VM = 32, N_RED = 18, N_CHAIN = 82, N_QX = 16, N_OUT = 18, N_ZX = 32, N_FFN = 96;
constexpr int P0_ROWS = 1792;
constexpr unsigned SPIN_CAP = 1u << 17;
constexpr unsigned MB2 = 2u << 20;
constexpr unsigned OFF_MEM = 0;
constexpr unsigned LOG_FLAGS = 640u << 10;
constexpr unsigned OFF_PM = MB2;
constexpr unsigned OFF_AB = 3 * MB2;
constexpr unsigned AB_BYTES = S_TOK * DM * 2;
constexpr unsigned LOG_ZM = 704u << 10;
constexpr unsigned ZM_SLOT = 36 * 8192;
constexpr unsigned OFF_CTL = OFF_AB + AB_BYTES;
constexpr unsigned FILL_END = OFF_CTL;
constexpr unsigned OFF_XN = 5 * MB2;
constexpr unsigned PX_SLOT = 32 * 2 * 1280;
constexpr unsigned OFF_PART = 8 * MB2;
constexpr unsigned OFF_HP = 22 * MB2;
constexpr unsigned OFF_Z = 31 * MB2;
constexpr unsigned Z_SLOT = 36 * 32768;
constexpr unsigned OFF_ZH = 68 * MB2;
constexpr unsigned OFF_PX = 78 * MB2;
constexpr unsigned OFF_QX = 81 * MB2;
constexpr unsigned WS_SPAN = OFF_QX + 64u * 32 * 8192;
constexpr unsigned FL_ZX = LOG_FLAGS, FL_ZM = LOG_FLAGS + 1024, FL_PX = LOG_FLAGS + 2048, FL_QX = LOG_FLAGS + 3072;
constexpr int LDS_BYTES = 160 * 1024;

DEV unsigned nearmap(unsigned o, unsigned par) { return ((o >> 13) << 14) + (par << 13) + (o & 8191u); }
DEV unsigned pk(float a, float b) { f2 v = {a, b}; return __builtin_bit_cast(unsigned, __builtin_convertvector(v, bfx2)); }
DEV float bflo(unsigned u) { return __uint_as_float(u << 16); }
DEV float bfhi(unsigned u) { return __uint_as_float(u & 0xffff0000u); }
DEV float rbf(float a) { return bflo(pk(a, a)); }
DEV unsigned perm_lo(unsigned a, unsigned b) { return __builtin_amdgcn_perm(b, a, 0x05040100u); }
DEV unsigned perm_hi(unsigned a, unsigned b) { return __builtin_amdgcn_perm(b, a, 0x07060302u); }
DEV f4 mfma(u4 a, u4 b, f4 c) { return __builtin_amdgcn_mfma_f32_16x16x32_bf16(__builtin_bit_cast(bf8, a), __builtin_bit_cast(bf8, b), c, 0, 0, 0); }
DEV f4 smf(u4 a, u8v b, f4 c, int idx) { return __builtin_amdgcn_smfmac_f32_16x16x64_bf16(__builtin_bit_cast(bf8, a), __builtin_bit_cast(bf16x16, b), c, idx, 0, 0); }
DEV u4 lo4(u8v b) { return __builtin_shufflevector(b, b, 0, 1, 2, 3); }
DEV u4 hi4(u8v b) { return __builtin_shufflevector(b, b, 4, 5, 6, 7); }
DEV u8v cat8(u4 a, u4 b) { return __builtin_shufflevector(a, b, 0, 1, 2, 3, 4, 5, 6, 7); }
DEV float fold32(float a, float b) {
    auto r = __builtin_amdgcn_permlane32_swap(__float_as_uint(a), __float_as_uint(b), false, false);
    return __uint_as_float(r[0]) + __uint_as_float(r[1]);
}
DEV float xmax32(float v) { auto r = __builtin_amdgcn_permlane32_swap(__float_as_uint(v), __float_as_uint(v), false, false); return fmaxf(__uint_as_float(r[0]), __uint_as_float(r[1])); }
DEV float xsum32(float v) { auto r = __builtin_amdgcn_permlane32_swap(__float_as_uint(v), __float_as_uint(v), false, false); return __uint_as_float(r[0]) + __uint_as_float(r[1]); }
DEV float xmax16(float v) { auto r = __builtin_amdgcn_permlane16_swap(__float_as_uint(v), __float_as_uint(v), false, false); return fmaxf(__uint_as_float(r[0]), __uint_as_float(r[1])); }
DEV float xsum16(float v) { auto r = __builtin_amdgcn_permlane16_swap(__float_as_uint(v), __float_as_uint(v), false, false); return __uint_as_float(r[0]) + __uint_as_float(r[1]); }
DEV float ror8(float v) { return __uint_as_float(__builtin_amdgcn_update_dpp(0u, __float_as_uint(v), 0x128, 0xf, 0xf, false)); }

#define STAMP(role_id, s, k, cond)
typedef __amdgpu_buffer_rsrc_t rsrc_t;
DEV u4 ld16c(rsrc_t r, unsigned off) { asm volatile("" ::: "memory"); return __builtin_amdgcn_raw_buffer_load_b128(r, off, 0, 16); }
DEV u2 ld8c(rsrc_t r, unsigned off) { asm volatile("" ::: "memory"); return __builtin_amdgcn_raw_buffer_load_b64(r, off, 0, 16); }
DEV unsigned ld4c(rsrc_t r, unsigned off) { asm volatile("" ::: "memory"); return __builtin_amdgcn_raw_buffer_load_b32(r, off, 0, 16); }
DEV void st16c(rsrc_t r, unsigned off, u4 v) { __builtin_amdgcn_raw_buffer_store_b128(v, r, off, 0, 16); }
DEV void st8c(rsrc_t r, unsigned off, u2 v) { __builtin_amdgcn_raw_buffer_store_b64(v, r, off, 0, 16); }
DEV void st4c(rsrc_t r, unsigned off, unsigned v) { __builtin_amdgcn_raw_buffer_store_b32(v, r, off, 0, 16); }
DEV void drain() { asm volatile("s_waitcnt vmcnt(0)" ::: "memory"); }
DEV void bar() { asm volatile("s_waitcnt lgkmcnt(0)\n\ts_barrier" ::: "memory"); }
DEV bool full4(u4 v) { return (v.x != ~0u) & (v.y != ~0u) & (v.z != ~0u) & (v.w != ~0u); }
DEV bool full2(u2 v) { return (v.x != ~0u) & (v.y != ~0u); }
DEV unsigned sane(unsigned v) { return v == ~0u ? 0x7fff7fffu : v; }
DEV u4 lds16(const char* p) { return *(const LAS u4*)(p); }
DEV int aperm(int g, int e) { return 16 * (g >> 1) + 4 * (g & 1) + 8 * ((e >> 1) & 1) + 2 * (e >> 2) + (e & 1); }

__global__ __launch_bounds__(256) void k_pro(const unsigned short* __restrict__ x, const unsigned short* __restrict__ mem0, char* __restrict__ ws) {
    const int tid = threadIdx.x, lane = tid & 63, wave = tid >> 6, b = blockIdx.x;
    __shared__ __attribute__((aligned(16))) unsigned short rowbuf[4][2][DM];
    {
        const u4 ones = {~0u, ~0u, ~0u, ~0u}, zero = {0, 0, 0, 0};
        for (unsigned o = (b * 256 + tid) * 16; o < FILL_END; o += 256 * 256 * 16) {
            if (o < MB2) {
                unsigned lg = ((o >> 14) << 13) + (o & 8191u);
                if (lg < 9216) continue;
                if (lg >= LOG_FLAGS + 4096) continue;
                *(u4*)(ws + o) = (lg >= LOG_FLAGS) ? zero : ones;
            } else if (o < 3 * MB2) {
                const unsigned q = o & (MB2 - 1);
                if ((q >= 2 * 32 * 20480 && q < 2 * LOG_ZM) || q >= 2 * (LOG_ZM + ZM_SLOT)) continue;
                *(u4*)(ws + o) = ones;
            } else *(u4*)(ws + o) = ones;
        }
    }
    if (b == 1) {
        for (int i = tid; i < 576; i += 256) {
            int t = i >> 6, l = i & 63, r = l & 7;
            u4 v = *(const u4*)(mem0 + r * DM + 64 * t + 8 * (l >> 3));
            v.x = sane(v.x); v.y = sane(v.y); v.z = sane(v.z); v.w = sane(v.w);
            *(u4*)(ws + OFF_MEM + nearmap(i * 16, 0)) = v;
            *(u4*)(ws + OFF_MEM + nearmap(i * 16, 1)) = v;
        }
        if (tid < 192) *(u4*)(ws + OFF_CTL + 1024 + tid * 16) = u4{0, 0, 0, 0};
    }
    #pragma unroll
    for (int rr = 0; rr < 2; ++rr) {
        const int row = b * 8 + wave * 2 + rr;
        float v[9]; float s = 0.f;
        #pragma unroll
        for (int i = 0; i < 9; ++i) { v[i] = __uint_as_float((unsigned)x[row * DM + lane + 64 * i] << 16); s += v[i]; }
        #pragma unroll
        for (int o = 32; o; o >>= 1) s += __shfl_xor(s, o);
        const float mean = s * (1.f / DM);
        float q = 0.f;
        #pragma unroll
        for (int i = 0; i < 9; ++i) { float d = v[i] - mean; q += d * d; }
        #pragma unroll
        for (int o = 32; o; o >>= 1) q += __shfl_xor(q, o);
        const float mb = rbf(mean), vb = rbf(q * (1.f / DM));
        const float sd = rbf(sqrtf(rbf(vb + 1e-5f)));
        #pragma unroll
        for (int i = 0; i < 9; ++i) rowbuf[wave][rr][lane + 64 * i] = (unsigned short)(pk(rbf(v[i] - mb) / sd, 0.f) & 0xffffu);
    }
    __syncthreads();
    for (int i = tid; i < 8 * 72; i += 256) {
        const int lr = i / 72, ch = i % 72, ks = ch >> 2, g = ch & 3, row = b * 8 + lr;
        const unsigned short* rb = rowbuf[lr >> 1][lr & 1] + 32 * ks + 16 * (g >> 1) + 4 * (g & 1);
        u2 p0 = *(const u2*)rb, p1 = *(const u2*)(rb + 8);
        u4 o = {p0.x, p1.x, p0.y, p1.y};
        *(u4*)(ws + OFF_XN + ((row >> 4) * 18 + ks) * 1024 + (g * 16 + (row & 15)) * 16) = o;
    }
    if (b == 0 && lane == 0 && wave < 3) {
        unsigned lat[2] = {~0u, ~0u};
        for (int it = 0; it < 14; ++it) {
            const int par = it & 1;
            GAS unsigned* p = (GAS unsigned*)(ws + wave * MB2 + MB2 - 16384 + par * 8192);
            unsigned xv, zero = 0;
            const unsigned long long c0 = __builtin_amdgcn_s_memtime();
            asm volatile("global_atomic_add %0, %1, %2, off sc0\n\ts_waitcnt vmcnt(0)" : "=&v"(xv) : "v"(p), "v"(zero) : "memory");
            const unsigned d = (unsigned)(__builtin_amdgcn_s_memtime() - c0);
            lat[par] = d < lat[par] ? d : lat[par];
        }
        *(unsigned*)(ws + OFF_CTL + wave * 4) = lat[1] < lat[0] ? 1u : 0u;
    }
}
struct Ctx { rsrc_t rw; const char* ws; char* sm; unsigned par0, par1, par2; int lane, wave; bool dead; };

DEV void stage_mem(Ctx& c, int s, char* mems) {
    const unsigned lb = s * 9216 + c.lane * 16;
    const unsigned o0 = OFF_MEM + nearmap(lb + c.wave * 1024, c.par0), o1 = OFF_MEM + nearmap(lb + (c.wave + 4) * 1024, c.par0);
    const unsigned o2 = OFF_MEM + nearmap(lb + 8 * 1024, c.par0);
    u4 x0, x1, x2;
    unsigned spins = c.dead ? SPIN_CAP : 0;
    if (c.wave == 0) {
        for (;;) {
            x0 = ld16c(c.rw, o0); x1 = ld16c(c.rw, o1); x2 = ld16c(c.rw, o2);
            if (__all(full4(x0) & full4(x1) & full4(x2))) break;
            if (++spins > SPIN_CAP) { c.dead = true; break; }
        }
        *(LAS u4*)(mems + 8192 + c.lane * 16) = x2;
    } else {
        for (;;) {
            x0 = ld16c(c.rw, o0); x1 = ld16c(c.rw, o1);
            if (__all(full4(x0) & full4(x1))) break;
            if (++spins > SPIN_CAP) { c.dead = true; break; }
        }
    }
    *(LAS u4*)(mems + c.wave * 1024 + c.lane * 16) = x0;
    *(LAS u4*)(mems + (c.wave + 4) * 1024 + c.lane * 16) = x1;
    bar();
}
DEV void wait_flags(Ctx& c, unsigned off, unsigned want) {
    unsigned spins = c.dead ? SPIN_CAP : 0;
    for (;;) {
        unsigned v = ld4c(c.rw, off);
        if (__all(v >= want)) break;
        __builtin_amdgcn_s_sleep(6);
        if (++spins > SPIN_CAP) { c.dead = true; break; }
    }
}
DEV void xn_load(const Ctx& c, int seg, u4 (&xr)[9]) {
    const u4* p = (const u4*)(c.ws + OFF_XN + seg * 36864 + c.wave * 9216) + c.lane;
    #pragma unroll
    for (int i = 0; i < 9; ++i) xr[i] = p[i * 64];
}
DEV void xn_put(const Ctx& c, char* xnb, const u4 (&xr)[9]) {
    #pragma unroll
    for (int i = 0; i < 9; ++i) *(LAS u4*)(xnb + c.wave * 9216 + i * 1024 + c.lane * 16) = xr[i];
}
DEV void xn_dma(const Ctx& c, int seg, unsigned lds_off) {
    const char* src = c.ws + OFF_XN + seg * 36864 + c.wave * 9216 + c.lane * 16;
    const unsigned dst = (unsigned)(uintptr_t)(LAS char*)c.sm + lds_off + c.wave * 9216;
    #pragma unroll
    for (int i = 0; i < 9; ++i) {
        unsigned keep;
        asm volatile("s_mov_b32 %0, m0\n\ts_mov_b32 m0, %2\n\ts_nop 0\n\tglobal_load_lds_dwordx4 %1, off\n\ts_mov_b32 m0, %0"
                     : "=&s"(keep) : "v"(src + i * 1024), "s"(dst + i * 1024) : "memory");
    }
}
constexpr float SC_L2E = 0.08838834764831845f * 1.4426950408889634f;
DEV void smfA(f4& c, u4 a, const u8v& w, int idx) { asm volatile("v_smfmac_f32_16x16x64_bf16 %0, %1, %2, %3" : "+v"(c) : "v"(a), "a"(w), "v"(idx)); }
DEV void smfV(f4& c, u4 a, const u8v& w, int idx) { asm volatile("v_smfmac_f32_16x16x64_bf16 %0, %1, %2, %3" : "+v"(c) : "v"(a), "v"(w), "v"(idx)); }
DEV void mfA(f4& c, u4 a, u4 w) { asm volatile("v_mfma_f32_16x16x32_bf16 %0, %1, %2, %0" : "+v"(c) : "v"(a), "a"(w)); }
DEV void mfV(f4& c, u4 a, u4 w) { asm volatile("v_mfma_f32_16x16x32_bf16 %0, %1, %2, %0" : "+v"(c) : "v"(a), "v"(w)); }
DEV void pre2(f4& a, f4& b) { asm volatile("s_nop 1" : "+v"(a), "+v"(b)); }
DEV void post2(f4& a, f4& b) { asm volatile("s_nop 7\n\ts_nop 2" : "+v"(a), "+v"(b)); }
DEV void pin2(f4& a, f4& b) { asm volatile("" : "+v"(a), "+v"(b)); }

DEV void load_proj(const Ctx& c, const unsigned short* __restrict__ W, u8v (&w0)[9], u8v (&w1)[9]) {
    const int tid = c.wave * 64 + c.lane, cc = c.lane & 15, g = c.lane >> 4;
    u4 r[36];
    const unsigned short* src = W + (tid >> 4) * 4096 + (tid & 15) * 8;
    #pragma unroll
    for (int i = 0; i < 36; ++i) r[i] = *(const u4*)(src + i * 16 * 4096);
    bar();
    #pragma unroll
    for (int i = 0; i < 36; ++i) *(LAS u4*)(c.sm + (i * 16 + (tid >> 4)) * 272 + (tid & 15) * 16) = r[i];
    bar();
    const int q = cc >> 2, p = cc & 3;
    const char* tb = c.sm + (16 * (g >> 1) + 4 * (g & 1) + (q & 1) + 8 * (q >> 1)) * 272 + (c.wave * 32 + 4 * p) * 2;
    #pragma unroll
    for (int t = 0; t < 9; ++t)
        #pragma unroll
        for (int k = 0; k < 4; ++k) {
            const char* a = tb + (64 * t + 32 * (k >> 1) + 2 * (k & 1)) * 272;
            const u2 v0 = __builtin_bit_cast(u2, __builtin_amdgcn_ds_read_tr16_b64_v4i16((LAS s4v*)a));
            const u2 v1 = __builtin_bit_cast(u2, __builtin_amdgcn_ds_read_tr16_b64_v4i16((LAS s4v*)(a + 32)));
            w0[t][2 * k] = v0.x; w0[t][2 * k + 1] = v0.y; w1[t][2 * k] = v1.x; w1[t][2 * k + 1] = v1.y;
        }
}

DEV void role_qk(Ctx& c, int h, const unsigned short* __restrict__ Wq, const unsigned short* __restrict__ Wk) {
    const int lane = c.lane, wave = c.wave, cc = lane & 15, g = lane >> 4;
    char* const MEMS = c.sm + 73728; char* const QM = c.sm + 82944; char* const KM = QM + 2176;
    char* const KX = KM + 2176;
    const int sidx = (lane & 8) ? 0xEEEE : 0x4444;
    u8v q0[9], q1[9], k0[9], k1[9];
    load_proj(c, Wq + h * DH, q0, q1);
    load_proj(c, Wk + h * DH, k0, k1);
    const int xw = (wave * 16 + cc) * 4 + g * 4 * 272;
    const int mw = (wave * 16 + cc) * 4 + ((g & 1) * 4 + (lane >> 5) * 2) * 272;
    const unsigned pxflag = OFF_MEM + nearmap(FL_PX + (h * 2 + (wave - 1)) * 4, c.par0);
    bar();
    xn_dma(c, 0, 0);
    drain();
    bar();
    u4 q[4] = {};
    unsigned qflag = 0;
    const unsigned fqo = OFF_MEM + nearmap(FL_QX + (h * 2 + ((wave - 1) & 1)) * 4, c.par0);
    for (int s = -1; s < NSEG; ++s) {
        if (s >= 0) {
            u4 kf[8];
            if (wave < 3) {
                #pragma unroll
                for (int i = 0; i < 8; ++i) kf[i] = lds16(KX + ((i >> 2) * 16 + cc) * 272 + (i & 3) * 64 + g * 16);
            }
            const unsigned pb = OFF_PM + (s >> 5) * MB2, par = (s >> 5) ? c.par2 : c.par1, lb = (s & 31) * 20480;
            const unsigned po0 = pb + nearmap(lb + h * 512 + ((g >> 1) * 16 + cc) * 16 + (g & 1) * 8, par);
            const unsigned po1 = pb + nearmap(lb + 16384 + h * 128 + cc * 8 + (g & 1) * 4, par);
            f4 aq0 = {}, aq1 = {}, ak0 = {}, ak1 = {};
            pin2(aq0, aq1); pin2(ak0, ak1);
            stage_mem(c, s, MEMS);
            STAMP(h, s, 0, lane == 0 && wave == 0);
            u4 a[9];
            #pragma unroll
            for (int t = 0; t < 9; ++t) a[t] = lds16(MEMS + t * 1024 + lane * 16);
            #pragma unroll
            for (int t = 0; t < 9; ++t) {
                smfA(aq0, a[t], q0[t], sidx); smfA(aq1, a[t], q1[t], sidx);
                if (t < 7) { smfA(ak0, a[t], k0[t], sidx); smfA(ak1, a[t], k1[t], sidx); }
                else { smfV(ak0, a[t], k0[t], sidx); smfV(ak1, a[t], k1[t], sidx); }
            }
            post2(aq0, aq1); pin2(ak0, ak1);
            #pragma unroll
            for (int r = 0; r < 2; ++r) {
                *(LAS unsigned*)(QM + mw + r * 272) = pk(fold32(aq0[r], aq0[r + 2]), fold32(aq1[r], aq1[r + 2]));
                *(LAS unsigned*)(KM + mw + r * 272) = pk(fold32(ak0[r], ak0[r + 2]), fold32(ak1[r], ak1[r + 2]));
            }
            bar();
            STAMP(h, s, 3, lane == 0 && wave == 0);
            if (wave < 3 && (wave > 0 || s < NSEG - 1)) {
                f4 s0 = {}, s1 = {}, sm = {};
                u4 km[4];
                if (wave == 0) {
                    #pragma unroll
                    for (int ks = 0; ks < 4; ++ks) q[ks] = lds16(QM + (lane & 7) * 272 + ks * 64 + g * 16);
                }
                #pragma unroll
                for (int ks = 0; ks < 4; ++ks) km[ks] = lds16(KM + (lane & 7) * 272 + ks * 64 + g * 16);
                #pragma unroll
                for (int ks = 0; ks < 4; ++ks) { s0 = mfma(kf[ks], q[ks], s0); s1 = mfma(kf[4 + ks], q[ks], s1); sm = mfma(km[ks], q[ks], sm); }
                const float NEG = -3.0e38f;
                if (wave == 0) {
                    const bool hh = lane & 8;
                    float v0 = hh ? s0[2] : s0[0], v1 = hh ? s0[3] : s0[1], v2 = hh ? s1[2] : s1[0], v3 = hh ? s1[3] : s1[1];
                    float v4 = hh ? sm[2] : sm[0], v5 = hh ? sm[3] : sm[1];
                    if (g >= 2) { v4 = NEG; v5 = NEG; }
                    float m = fmaxf(fmaxf(fmaxf(v0, v1), fmaxf(v2, v3)), fmaxf(v4, v5));
                    m = fmaxf(m, ror8(m)); m = xmax16(m); m = xmax32(m);
                    const float mc = m * SC_L2E;
                    v0 = __builtin_amdgcn_exp2f(v0 * SC_L2E - mc); v1 = __builtin_amdgcn_exp2f(v1 * SC_L2E - mc);
                    v2 = __builtin_amdgcn_exp2f(v2 * SC_L2E - mc); v3 = __builtin_amdgcn_exp2f(v3 * SC_L2E - mc);
                    v4 = __builtin_amdgcn_exp2f(v4 * SC_L2E - mc); v5 = __builtin_amdgcn_exp2f(v5 * SC_L2E - mc);
                    if (g >= 2) { v4 = 0.f; v5 = 0.f; }
                    float l = ((v0 + v1) + (v2 + v3)) + (v4 + v5);
                    l += ror8(l); l = xsum16(l); l = xsum32(l);
                    const float inv = __builtin_amdgcn_rcpf(l);
                    u2 o = {pk(v0 * inv, v1 * inv) & 0x7fff7fffu, pk(v2 * inv, v3 * inv) & 0x7fff7fffu};
                    st8c(c.rw, po0, o);
                    if (g < 2) st4c(c.rw, po1, pk(v4 * inv, v5 * inv) & 0x7fff7fffu);
                    STAMP(h, s, 1, lane == 0);
                } else {
                    const int qi = (wave - 1) * 16 + cc;
                    float v[12];
                    #pragma unroll
                    for (int r = 0; r < 4; ++r) {
                        v[r] = (4 * g + r <= qi) ? s0[r] : NEG; v[4 + r] = (16 + 4 * g + r <= qi) ? s1[r] : NEG; v[8 + r] = (g < 2) ? sm[r] : NEG;
                    }
                    float m = v[0];
                    #pragma unroll
                    for (int r = 1; r < 12; ++r) m = fmaxf(m, v[r]);
                    m = xmax16(m); m = xmax32(m);
                    const float mc = m * SC_L2E;
                    float l = 0.f;
                    #pragma unroll
                    for (int r = 0; r < 12; ++r) { float e = __builtin_amdgcn_exp2f(v[r] * SC_L2E - mc); v[r] = (v[r] == NEG) ? 0.f : e; l += v[r]; }
                    l = xsum16(l); l = xsum32(l);
                    const float inv = __builtin_amdgcn_rcpf(l);
                    const unsigned px = OFF_PX + s * PX_SLOT + (h * 2 + wave - 1) * 1280;
                    u4 o = {pk(v[0] * inv, v[1] * inv), pk(v[2] * inv, v[3] * inv), pk(v[4] * inv, v[5] * inv), pk(v[6] * inv, v[7] * inv)};
                    st16c(c.rw, px + lane * 16, o);
                    if (g < 2) st8c(c.rw, px + 1024 + cc * 16 + g * 8, u2{pk(v[8] * inv, v[9] * inv), pk(v[10] * inv, v[11] * inv)});
                }
            }
        }
        const bool pxw = (wave == 1 || wave == 2) && s >= 0;
        if (s == NSEG - 1) { if (pxw) { drain(); if (lane == 0) st4c(c.rw, pxflag, s + 1); } break; }
        STAMP(h, s, 4, lane == 0 && wave == 3);
        const char* XNB = c.sm + ((s + 1) & 1) * 36864;
        f4 acc[2][4] = {};
        #pragma unroll
        for (int i = 2; i < 4; ++i) pre2(acc[0][i], acc[1][i]);
        u4 n0 = lds16(XNB + lane * 16), n1 = lds16(XNB + 18 * 1024 + lane * 16);
        #pragma unroll
        for (int ks = 0; ks < 18; ++ks) {
            const u4 a0 = n0, a1 = n1;
            if (ks < 17) { n0 = lds16(XNB + (ks + 1) * 1024 + lane * 16); n1 = lds16(XNB + (19 + ks) * 1024 + lane * 16); }
            const int t = ks >> 1;
            const u4 b2 = (ks & 1) ? hi4(k0[t]) : lo4(k0[t]), b3 = (ks & 1) ? hi4(k1[t]) : lo4(k1[t]);
            if (t < 7) { mfA(acc[0][2], a0, b2); mfA(acc[1][2], a1, b2); mfA(acc[0][3], a0, b3); mfA(acc[1][3], a1, b3); }
            else { mfV(acc[0][2], a0, b2); mfV(acc[1][2], a1, b2); mfV(acc[0][3], a0, b3); mfV(acc[1][3], a1, b3); }
            if (ks == 8) {
                if (pxw) { drain(); if (lane == 0) st4c(c.rw, pxflag, s + 1); }
                if (wave == 1 || wave == 2) {
                    if (!__all(qflag >= (unsigned)(s + 2))) wait_flags(c, fqo, s + 2);
                    #pragma unroll
                    for (int i = 0; i < 4; ++i) q[i] = ld16c(c.rw, OFF_QX + (((s + 1) * 32 + h) * 2 + wave - 1) * 4096 + i * 1024 + lane * 16);
                    qflag = ld4c(c.rw, fqo);
                }
                if (s + 2 < NSEG) xn_dma(c, s + 2, (s & 1) * 36864);
            }
        }
        STAMP(h, s, 5, lane == 0 && wave == 3);
        bar();
        STAMP(h, s, 7, lane == 0 && wave == 3);
        post2(acc[0][3], acc[1][3]);
        pin2(acc[0][2], acc[1][2]);
        #pragma unroll
        for (int rt = 0; rt < 2; ++rt)
            #pragma unroll
            for (int r = 0; r < 4; ++r) {
                *(LAS unsigned*)(KX + xw + (rt * 16 + r) * 272) = pk(acc[rt][2][r], acc[rt][3][r]);
            }
        bar();
        STAMP(h, s, 2, lane == 0 && wave == 0);
    }
}

DEV void load_vo(const Ctx& c, int h, const unsigned short* __restrict__ Wv, const unsigned short* __restrict__ Wo, u8v (&v0)[9], u8v (&v1)[9], u8v (&wo)[2][9]) {
    const int lane = c.lane, wave = c.wave, cc = lane & 15, g = lane >> 4, tid = wave * 64 + lane;
    load_proj(c, Wv + h * DH, v0, v1);
    {
        const u4* src = (const u4*)(Wo + (size_t)h * DH * DM);
        u4 r[36];
        #pragma unroll
        for (int i = 0; i < 36; ++i) r[i] = src[i * 256 + tid];
        bar();
        #pragma unroll
        for (int i = 0; i < 36; ++i) { const int ch = i * 256 + tid; *(LAS u4*)(c.sm + (ch / 72) * 1168 + (ch % 72) * 16) = r[i]; }
        bar();
        const int q = cc >> 2, p = cc & 3;
        const char* tb = c.sm + (8 * (g >> 1) + 2 * (g & 1) + 4 * (q >> 1) + 16 * (q & 1)) * 1168 + (wave * 144 + 4 * p) * 2;
        #pragma unroll
        for (int t = 0; t < 2; ++t)
            #pragma unroll
            for (int u = 0; u < 9; ++u)
                #pragma unroll
                for (int k = 0; k < 4; ++k) {
                    const u2 w2 = __builtin_bit_cast(u2, __builtin_amdgcn_ds_read_tr16_b64_v4i16((LAS s4v*)(tb + (64 * t + 32 * (k >> 1) + (k & 1)) * 1168 + u * 32)));
                    wo[t][u][2 * k] = w2.x; wo[t][u][2 * k + 1] = w2.y;
                }
    }
}
DEV void role_vm(Ctx& c, int h, const unsigned short* __restrict__ Wv, const unsigned short* __restrict__ Wo) {
    const int lane = c.lane, wave = c.wave, cc = lane & 15, g = lane >> 4, tid = wave * 64 + lane;
    char* const MEMS = c.sm; char* const VM = c.sm + 9216;
    const int sidx = (lane & 8) ? 0xEEEE : 0x4444;
    u8v v0[9], v1[9], wo[2][9];
    load_vo(c, h, Wv, Wo, v0, v1, wo);
    char* const MW = VM + (wave * 16 + cc) * 4 + ((g & 1) * 4 + (lane >> 5) * 2) * 272;
    bar();
    for (int s = 0; s < NSEG; ++s) {
            f4 a0 = {}, a1 = {}, z[9] = {};
            pin2(a0, a1);
            #pragma unroll
            for (int u = 0; u < 8; u += 2) pin2(z[u], z[u + 1]);
            pin2(z[8], z[0]);
            const unsigned zl = (wave * 9 + (lane >> 5)) * 8192 + h * 256 + cc * 16 + (g & 1) * 8;
            const unsigned zb = OFF_PM + (s & 1) * MB2 + nearmap(LOG_ZM + zl, (s & 1) ? c.par2 : c.par1);
            stage_mem(c, s, MEMS);
            STAMP(32 + h, s, 0, tid == 0);
            u4 a[9];
            #pragma unroll
            for (int t = 0; t < 9; ++t) a[t] = lds16(MEMS + t * 1024 + lane * 16);
            #pragma unroll
            for (int t = 0; t < 9; ++t) { smfA(a0, a[t], v0[t], sidx); smfA(a1, a[t], v1[t], sidx); }
            post2(a0, a1);
            #pragma unroll
            for (int r = 0; r < 2; ++r) *(LAS unsigned*)(MW + r * 272) = pk(fold32(a0[r], a0[r + 2]), fold32(a1[r], a1[r + 2]));
            bar();
            const u4 va = lds16(VM + (lane & 7) * 272 + (lane >> 3) * 16), vb = lds16(VM + (lane & 7) * 272 + 128 + (lane >> 3) * 16);
            #pragma unroll
            for (int u = 0; u < 9; ++u) { smfA(z[u], va, wo[0][u], sidx); if (u < 5) smfA(z[u], vb, wo[1][u], sidx); else smfV(z[u], vb, wo[1][u], sidx); }
            post2(z[8], z[7]);
            u2 zo[5];
            #pragma unroll
            for (int u = 0; u < 8; u += 2) {
                pin2(z[u], z[u + 1]);
                zo[u >> 1] = u2{sane(pk(fold32(z[u][0], z[u + 1][0]), fold32(z[u][1], z[u + 1][1]))), sane(pk(fold32(z[u][2], z[u + 1][2]), fold32(z[u][3], z[u + 1][3])))};
                if (s < NSEG - 1) st8c(c.rw, zb + u * 16384, zo[u >> 1]);
            }
            zo[4] = u2{sane(pk(fold32(z[8][0], z[8][0]), fold32(z[8][1], z[8][1]))), sane(pk(fold32(z[8][2], z[8][2]), fold32(z[8][3], z[8][3])))};
            if (lane < 32 && s < NSEG - 1) st8c(c.rw, zb + 8 * 16384, zo[4]);
            const unsigned zh = OFF_ZH + s * ZM_SLOT + zl;
            #pragma unroll
            for (int u = 0; u < 4; ++u) st8c(c.rw, zh + u * 16384, zo[u]);
            if (lane < 32) st8c(c.rw, zh + 8 * 8192, zo[4]);
            STAMP(32 + h, s, 1, tid == 0);
            drain();
            if (lane == 0) st4c(c.rw, OFF_MEM + nearmap(FL_ZM + (wave * 32 + h) * 4, c.par0), s + 1);
    }
}
DEV void role_zx(Ctx& c, int h, const unsigned short* __restrict__ Wv, const unsigned short* __restrict__ Wo) {
    const int lane = c.lane, wave = c.wave, cc = lane & 15, g = lane >> 4, tid = wave * 64 + lane;
    char* const XNB = c.sm; char* const VX = c.sm + 36864;
    u8v v0[9], v1[9], wo[2][9];
    load_vo(c, h, Wv, Wo, v0, v1, wo);
    u4 xr[9];
    xn_load(c, 0, xr);
    bar();
    xn_put(c, XNB, xr);
    xn_load(c, 1, xr);
    char* const XW = VX + (wave * 16 + 8 * (cc >> 3) + 4 * ((cc >> 1) & 1) + ((cc >> 2) & 1) + 2 * (cc & 1)) * 4 + g * 4 * 272;
    const unsigned fzx = OFF_MEM + nearmap(FL_ZX + (wave * 32 + h) * 4, c.par0);
    bar();
    for (int s = 0; s < NSEG; ++s) {
        f4 acc[2][2] = {};
        pre2(acc[0][0], acc[1][0]); pre2(acc[0][1], acc[1][1]);
        u4 n0 = lds16(XNB + lane * 16), n1 = lds16(XNB + 18 * 1024 + lane * 16);
        #pragma unroll
        for (int ks = 0; ks < 18; ++ks) {
            const u4 x0 = n0, x1 = n1;
            if (ks < 17) { n0 = lds16(XNB + (ks + 1) * 1024 + lane * 16); n1 = lds16(XNB + (19 + ks) * 1024 + lane * 16); }
            const u4 b0 = (ks & 1) ? hi4(v0[ks >> 1]) : lo4(v0[ks >> 1]), b1 = (ks & 1) ? hi4(v1[ks >> 1]) : lo4(v1[ks >> 1]);
            mfA(acc[0][0], x0, b0); mfA(acc[1][0], x1, b0); mfA(acc[0][1], x0, b1); mfA(acc[1][1], x1, b1);
        }
        if (s > 0) { drain(); if (lane == 0) st4c(c.rw, fzx, s); }
        bar();
        post2(acc[0][1], acc[1][1]); pin2(acc[0][0], acc[1][0]);
        #pragma unroll
        for (int rt = 0; rt < 2; ++rt)
            #pragma unroll
            for (int r = 0; r < 4; ++r) *(LAS unsigned*)(XW + (rt * 16 + r) * 272) = pk(acc[rt][0][r], acc[rt][1][r]);
        if (s + 1 < NSEG) xn_put(c, XNB, xr);
        bar();
        if (s + 2 < NSEG) xn_load(c, s + 2, xr);
        u4 vf[8];
        #pragma unroll
        for (int i = 0; i < 8; ++i) vf[i] = lds16(VX + ((i >> 2) * 16 + cc) * 272 + (i & 3) * 64 + g * 16);
        const unsigned zx = OFF_Z + s * Z_SLOT + wave * 9 * 32768 + h * 1024 + lane * 16;
        f4 z0[9] = {}, z1[9] = {};
        #pragma unroll
        for (int u = 0; u < 9; ++u) pre2(z0[u], z1[u]);
        #pragma unroll
        for (int u = 0; u < 9; ++u) {
            #pragma unroll
            for (int ks = 0; ks < 4; ++ks) {
                const u4 b = (ks & 1) ? hi4(wo[ks >> 1][u]) : lo4(wo[ks >> 1][u]);
                if (ks < 2 || u < 5) { mfA(z0[u], vf[ks], b); mfA(z1[u], vf[4 + ks], b); } else { mfV(z0[u], vf[ks], b); mfV(z1[u], vf[4 + ks], b); }
            }
            if (u > 0) {
                pin2(z0[u - 1], z1[u - 1]);
                st16c(c.rw, zx + (u - 1) * 32768, u4{pk(z0[u - 1][0], z0[u - 1][1]), pk(z0[u - 1][2], z0[u - 1][3]), pk(z1[u - 1][0], z1[u - 1][1]), pk(z1[u - 1][2], z1[u - 1][3])});
            }
        }
        post2(z0[8], z1[8]);
        st16c(c.rw, zx + 8 * 32768, u4{pk(z0[8][0], z0[8][1]), pk(z0[8][2], z0[8][3]), pk(z1[8][0], z1[8][1]), pk(z1[8][2], z1[8][3])});
        if (s == 0 || s == NSEG - 1) { drain(); if (lane == 0) st4c(c.rw, fzx, s + 1); }
        STAMP(32 + h, s, 3, tid == 0);
    }
}

DEV void role_red(Ctx& c, int o) {
    const int lane = c.lane, wave = c.wave, cc = lane & 15, g = lane >> 4, tid = wave * 64 + lane, j0 = 2 * o;
    char* const RM = c.sm;
    const int sidx = (lane & 8) ? 0xEEEE : 0x4444;
    const unsigned fzx = OFF_MEM + nearmap(FL_ZX + (((j0 + ((lane >> 3) & 1)) / 9) * 32 + wave * 8 + (lane & 7)) * 4, c.par0);
    const unsigned zb0 = OFF_Z + j0 * 32768 + wave * 8192 + lane * 16;
    for (int s = 0; s < NSEG - 1; ++s) {
        const unsigned zm = OFF_PM + (s & 1) * MB2 + nearmap(LOG_ZM + j0 * 8192 + (wave * 8 + g) * 256 + cc * 16, (s & 1) ? c.par2 : c.par1);
        const unsigned pb = OFF_PM + (s >> 5) * MB2, par = (s >> 5) ? c.par2 : c.par1, lb = (s & 31) * 20480;
        const unsigned o0 = pb + nearmap(lb + wave * 4096 + lane * 16, par);
        const unsigned o1 = pb + nearmap(lb + 16384 + (wave * 8 + 2 * g) * 128 + cc * 8, par);
        const unsigned om = OFF_MEM + nearmap((s + 1) * 9216 + (j0 + (wave & 1)) * 256 + lane * 4, c.par0);
        u4 zf[2][10];
        wait_flags(c, fzx, s + 1);
        #pragma unroll
        for (int i = 0; i < 8; ++i) { zf[0][i] = ld16c(c.rw, zb0 + s * Z_SLOT + i * 1024); zf[1][i] = ld16c(c.rw, zb0 + s * Z_SLOT + 32768 + i * 1024); }
        u4 p[4]; u2 m0, m1;
        #pragma unroll
        for (int i = 0; i < 4; ++i) p[i] = ld16c(c.rw, o0 + i * 1024);
        m0 = ld8c(c.rw, o1); m1 = ld8c(c.rw, o1 + 128);
        asm volatile("" :: "v"(p[0]), "v"(p[1]), "v"(p[2]), "v"(p[3]), "v"(m0), "v"(m1));
        STAMP(64 + o, s, 0, tid == 0);
        unsigned spins = c.dead ? SPIN_CAP : 0;
        for (;;) {
            zf[0][8] = ld16c(c.rw, zm); zf[0][9] = ld16c(c.rw, zm + 1024); zf[1][8] = ld16c(c.rw, zm + 16384); zf[1][9] = ld16c(c.rw, zm + 17408);
            if (__all(full4(zf[0][8]) & full4(zf[0][9]) & full4(zf[1][8]) & full4(zf[1][9]))) break;
            if (++spins > SPIN_CAP) { c.dead = true; break; }
        }
        STAMP(64 + o, s, 1, tid == 0);
        for (;;) {
            #pragma unroll
            for (int i = 0; i < 4; ++i) p[i] = ld16c(c.rw, o0 + i * 1024);
            m0 = ld8c(c.rw, o1); m1 = ld8c(c.rw, o1 + 128);
            if (__all(full4(p[0]) & full4(p[1]) & full4(p[2]) & full4(p[3]) & full2(m0) & full2(m1))) break;
            if (++spins > SPIN_CAP) { c.dead = true; break; }
        }
        STAMP(64 + o, s, 2, tid == 0);
        f4 a0 = {}, a1 = {};
        #pragma unroll
        for (int i = 0; i < 4; ++i) { a0 = smf(p[i], cat8(zf[0][2 * i], zf[0][2 * i + 1]), a0, sidx); a1 = smf(p[i], cat8(zf[1][2 * i], zf[1][2 * i + 1]), a1, sidx); }
        const u4 pm = {m0.x, m0.y, m1.x, m1.y};
        a0 = smf(pm, cat8(zf[0][8], zf[0][9]), a0, sidx); a1 = smf(pm, cat8(zf[1][8], zf[1][9]), a1, sidx);
        char* w = RM + (s & 1) * 4096 + wave * 512 + ((g & 1) * 4 + (lane >> 5) * 2) * 64 + cc * 4;
        *(LAS float*)(w) = fold32(a0[0], a0[2]); *(LAS float*)(w + 64) = fold32(a0[1], a0[3]);
        *(LAS float*)(w + 2048) = fold32(a1[0], a1[2]); *(LAS float*)(w + 2112) = fold32(a1[1], a1[3]);
        bar();
        if (wave < 2) {
            const char* rp = RM + (s & 1) * 4096 + wave * 2048 + ((lane >> 2) & 7) * 64 + (lane >> 5) * 32 + (lane & 3) * 8;
            const f2 v = (*(const LAS f2*)(rp) + *(const LAS f2*)(rp + 512)) + (*(const LAS f2*)(rp + 1024) + *(const LAS f2*)(rp + 1536));
            st4c(c.rw, om, sane(pk(v.x, v.y)));
            STAMP(64 + o, s, 3, tid == 0);
        }
        const u4 ones = {~0u, ~0u, ~0u, ~0u};
        st16c(c.rw, zm, ones); st16c(c.rw, zm + 1024, ones); st16c(c.rw, zm + 16384, ones); st16c(c.rw, zm + 17408, ones);
    }
}

DEV void role_qx(Ctx& c, int o, const unsigned short* __restrict__ Wq) {
    const int lane = c.lane, wave = c.wave, cc = lane & 15, g = lane >> 4;
    char* const QX = c.sm + 73728;
    u8v q0[9], q1[9], k0[9], k1[9];
    load_proj(c, Wq + 2 * o * DH, q0, q1);
    load_proj(c, Wq + (2 * o + 1) * DH, k0, k1);
    const int xw = (wave * 16 + cc) * 4 + g * 4 * 272;
    const unsigned fq = OFF_MEM + nearmap(FL_QX + (o * 4 + wave) * 4, c.par0);
    bar();
    xn_dma(c, 0, 0);
    drain();
    bar();
    for (int s = 0; s < NSEG; ++s) {
        const char* XNB = c.sm + (s & 1) * 36864;
        if (s + 1 < NSEG) xn_dma(c, s + 1, ((s + 1) & 1) * 36864);
        f4 acc[2][4] = {};
        #pragma unroll
        for (int i = 0; i < 4; ++i) pre2(acc[0][i], acc[1][i]);
        u4 n0 = lds16(XNB + lane * 16), n1 = lds16(XNB + 18 * 1024 + lane * 16);
        #pragma unroll
        for (int ks = 0; ks < 18; ++ks) {
            const u4 a0 = n0, a1 = n1;
            if (ks < 17) { n0 = lds16(XNB + (ks + 1) * 1024 + lane * 16); n1 = lds16(XNB + (19 + ks) * 1024 + lane * 16); }
            const int t = ks >> 1;
            const u4 b0 = (ks & 1) ? hi4(q0[t]) : lo4(q0[t]), b1 = (ks & 1) ? hi4(q1[t]) : lo4(q1[t]);
            const u4 b2 = (ks & 1) ? hi4(k0[t]) : lo4(k0[t]), b3 = (ks & 1) ? hi4(k1[t]) : lo4(k1[t]);
            mfA(acc[0][0], a0, b0); mfA(acc[1][0], a1, b0); mfA(acc[0][1], a0, b1); mfA(acc[1][1], a1, b1);
            if (t < 7) { mfA(acc[0][2], a0, b2); mfA(acc[1][2], a1, b2); mfA(acc[0][3], a0, b3); mfA(acc[1][3], a1, b3); }
            else { mfV(acc[0][2], a0, b2); mfV(acc[1][2], a1, b2); mfV(acc[0][3], a0, b3); mfV(acc[1][3], a1, b3); }
        }
        drain();
        if (s > 0 && lane == 0) st4c(c.rw, fq, s);
        bar();
        post2(acc[0][3], acc[1][3]);
        #pragma unroll
        for (int i = 0; i < 3; ++i) pin2(acc[0][i], acc[1][i]);
        #pragma unroll
        for (int rt = 0; rt < 2; ++rt)
            #pragma unroll
            for (int r = 0; r < 4; ++r) {
                *(LAS unsigned*)(QX + xw + (rt * 16 + r) * 272) = pk(acc[rt][0][r], acc[rt][1][r]);
                *(LAS unsigned*)(QX + 8704 + xw + (rt * 16 + r) * 272) = pk(acc[rt][2][r], acc[rt][3][r]);
            }
        bar();
        #pragma unroll
        for (int ks = 0; ks < 4; ++ks)
            st16c(c.rw, OFF_QX + ((s * 32 + 2 * o) * 2 + wave) * 4096 + ks * 1024 + lane * 16, lds16(QX + (wave >> 1) * 8704 + ((wave & 1) * 16 + cc) * 272 + ks * 64 + g * 16));
        if (s == 0 || s == NSEG - 1) { drain(); if (lane == 0) st4c(c.rw, fq, s + 1); }
    }
}

DEV void role_out(Ctx& c, int o, const unsigned short* __restrict__ x, unsigned short* __restrict__ out) {
    const int lane = c.lane, wave = c.wave, cc = lane & 15, g = lane >> 4, tid = wave * 64 + lane;
    char* const RX = c.sm;
    const int j0 = 2 * o;
    const unsigned fl = (g == 0) ? FL_PX + (wave * 16 + cc) * 4 : ((g == 1 ? FL_ZX : FL_ZM) + (((j0 + (cc >> 3)) / 9) * 32 + wave * 8 + (cc & 7)) * 4);
    const unsigned flo = OFF_MEM + nearmap(fl, c.par0);
    const int orow = tid >> 3, ocol = 32 * o + (tid & 7) * 4;
    for (int s = 0; s < NSEG; ++s) {
        const unsigned abo = OFF_AB + ((s * 32 + orow) * DM + ocol) * 2;
        const bool fin = s * 32 >= P0_ROWS;
        const u2 xv = *(const u2*)(x + (s * 32 + orow) * DM + ocol);
        wait_flags(c, flo, s + 1);
        STAMP(64 + o, s, 4, tid == 0);
        const unsigned xb = OFF_PX + s * PX_SLOT + wave * 8 * 2560;
        const unsigned zb = OFF_Z + s * Z_SLOT + j0 * 32768 + wave * 8192 + lane * 16;
        const unsigned zh = OFF_ZH + s * ZM_SLOT + j0 * 8192 + (wave * 8 + g) * 256 + cc * 16;
        u4 pa[20], zf[20];
        #pragma unroll
        for (int i = 0; i < 8; ++i) {
            pa[i] = ld16c(c.rw, xb + i * 2560 + lane * 16); pa[10 + i] = ld16c(c.rw, xb + i * 2560 + 1280 + lane * 16);
            zf[i] = ld16c(c.rw, zb + i * 1024); zf[10 + i] = ld16c(c.rw, zb + 32768 + i * 1024);
        }
        #pragma unroll
        for (int i = 0; i < 2; ++i) {
            pa[8 + i] = ld16c(c.rw, xb + (i * 4 + g) * 2560 + 1024 + cc * 16); pa[18 + i] = ld16c(c.rw, xb + (i * 4 + g) * 2560 + 1280 + 1024 + cc * 16);
            zf[8 + i] = ld16c(c.rw, zh + i * 1024); zf[18 + i] = ld16c(c.rw, zh + 8192 + i * 1024);
        }
        u2 bv = {0, 0};
        if (fin) bv = ld8c(c.rw, abo);
        __builtin_amdgcn_sched_barrier(0);
        f4 acc[2][2] = {};
        #pragma unroll
        for (int i = 0; i < 10; ++i)
            #pragma unroll
            for (int qt = 0; qt < 2; ++qt) { acc[qt][0] = mfma(pa[qt * 10 + i], zf[i], acc[qt][0]); acc[qt][1] = mfma(pa[qt * 10 + i], zf[10 + i], acc[qt][1]); }
        if (fin && !__all(full2(bv))) {
            unsigned spins = c.dead ? 8 * SPIN_CAP : 0;
            for (;;) { bv = ld8c(c.rw, abo); if (__all(full2(bv))) break; __builtin_amdgcn_s_sleep(8); if (++spins > 8 * SPIN_CAP) { c.dead = true; break; } }
        }
        char* const R = RX + (s & 1) * 18432;
        #pragma unroll
        for (int qt = 0; qt < 2; ++qt)
            #pragma unroll
            for (int r = 0; r < 4; ++r) {
                *(LAS float*)(R + wave * 4608 + (qt * 16 + g * 4 + r) * 144 + cc * 4) = acc[qt][0][r];
                *(LAS float*)(R + wave * 4608 + (qt * 16 + g * 4 + r) * 144 + 64 + cc * 4) = acc[qt][1][r];
            }
        bar();
        const char* rp = R + orow * 144 + (tid & 7) * 16;
        const f4 v = (*(const LAS f4*)(rp) + *(const LAS f4*)(rp + 4608)) + (*(const LAS f4*)(rp + 9216) + *(const LAS f4*)(rp + 13824));
        const unsigned a0 = pk(v[0], v[1]), a1 = pk(v[2], v[3]);
        if (fin) {
            const unsigned t0 = pk(bflo(xv.x) + bflo(a0), bfhi(xv.x) + bfhi(a0)), t1 = pk(bflo(xv.y) + bflo(a1), bfhi(xv.y) + bfhi(a1));
            *(u2*)(out + (s * 32 + orow) * DM + ocol) = u2{pk(bflo(t0) + bflo(bv.x), bfhi(t0) + bfhi(bv.x)), pk(bflo(t1) + bflo(bv.y), bfhi(t1) + bfhi(bv.y))};
        } else st8c(c.rw, abo, u2{sane(a0), sane(a1)});
        STAMP(64 + o, s, 5, tid == 0);
    }
}
DEV float gelu_t(float v) {
    const float u = 0.7978845608028654f * (v + 0.044715f * v * v * v);
    return v * __builtin_amdgcn_rcpf(1.f + __builtin_amdgcn_exp2f(-2.8853900817779268f * u));
}
DEV void mfC(f4& c, u4 a, u4 b) { asm volatile("v_mfma_f32_16x16x32_bf16 %0, %1, %2, %0" : "+a"(c) : "v"(a), "v"(b)); }
DEV void role_ffn(Ctx& c, int f, const unsigned short* __restrict__ x, const unsigned short* __restrict__ Wg, const unsigned short* __restrict__ Wu,
                  const unsigned short* __restrict__ Wd, unsigned short* __restrict__ out) {
    const int lane = c.lane, wave = c.wave, cc = lane & 15, g = lane >> 4, tid = wave * 64 + lane;
    const int p = f & 15, u = f >> 4;
    STAMP(100 + f, 0, 0, tid == 0);
    {
        const u4* src = (const u4*)(c.ws + OFF_XN + p * 147456) + tid;
        u4 r[36];
        #pragma unroll
        for (int i = 0; i < 36; ++i) r[i] = src[i * 256];
        __builtin_amdgcn_sched_barrier(0);
        #pragma unroll
        for (int i = 0; i < 36; ++i) *(LAS u4*)(c.sm + i * 4096 + tid * 16) = r[i];
    }
    bar();
    const unsigned hpark = OFF_HP + (unsigned)f * 196608 + tid * 8;
    const rsrc_t rg_ = __builtin_amdgcn_make_buffer_rsrc((void*)Wg, 0, DM * FF * 2, 0x00020000);
    const rsrc_t ru_ = __builtin_amdgcn_make_buffer_rsrc((void*)Wu, 0, DM * FF * 2, 0x00020000);
    const rsrc_t rd_ = __builtin_amdgcn_make_buffer_rsrc((void*)Wd, 0, DM * FF * 2, 0x00020000);
    #pragma unroll 1
    for (int ct = 0; ct < 3; ++ct) {
        const unsigned wo = (768 * u + 256 * ct + 64 * wave + 4 * cc + (16 * (g >> 1) + 4 * (g & 1)) * FF) * 2;
        f4 ag[8][4] = {}, au[8][4] = {};
        u2 rg[2][8], ru[2][8];
        #pragma unroll
        for (int z = 0; z < 2; ++z)
            #pragma unroll
            for (int e = 0; e < 8; ++e) {
                rg[z][e] = __builtin_amdgcn_raw_buffer_load_b64(rg_, wo, (32 * z + (e & 3) + 8 * (e >> 2)) * FF * 2, 0);
                ru[z][e] = __builtin_amdgcn_raw_buffer_load_b64(ru_, wo, (32 * z + (e & 3) + 8 * (e >> 2)) * FF * 2, 0);
            }
        #pragma unroll 1
        for (int k2 = 0; k2 < 18; k2 += 2) {
            #pragma unroll
            for (int z = 0; z < 2; ++z) {
                const int ks = k2 + z;
                u4 bg[4], bu[4];
                #pragma unroll
                for (int d = 0; d < 4; ++d) {
                    const int ia = (d & 1) * 4 + (d >> 1) * 2;
                    bg[0][d] = perm_lo(rg[z][ia].x, rg[z][ia + 1].x); bg[1][d] = perm_hi(rg[z][ia].x, rg[z][ia + 1].x);
                    bg[2][d] = perm_lo(rg[z][ia].y, rg[z][ia + 1].y); bg[3][d] = perm_hi(rg[z][ia].y, rg[z][ia + 1].y);
                    bu[0][d] = perm_lo(ru[z][ia].x, ru[z][ia + 1].x); bu[1][d] = perm_hi(ru[z][ia].x, ru[z][ia + 1].x);
                    bu[2][d] = perm_lo(ru[z][ia].y, ru[z][ia + 1].y); bu[3][d] = perm_hi(ru[z][ia].y, ru[z][ia + 1].y);
                }
                u4 a[8];
                #pragma unroll
                for (int rt = 0; rt < 8; ++rt) a[rt] = lds16(c.sm + (rt * 18 + ks) * 1024 + lane * 16);
                __builtin_amdgcn_sched_barrier(0);
                const int kn = ks < 16 ? ks + 2 : ks;
                #pragma unroll
                for (int e = 0; e < 8; ++e) {
                    rg[z][e] = __builtin_amdgcn_raw_buffer_load_b64(rg_, wo, (32 * kn + (e & 3) + 8 * (e >> 2)) * FF * 2, 0);
                    ru[z][e] = __builtin_amdgcn_raw_buffer_load_b64(ru_, wo, (32 * kn + (e & 3) + 8 * (e >> 2)) * FF * 2, 0);
                }
                __builtin_amdgcn_sched_barrier(0);
                #pragma unroll
                for (int rt = 0; rt < 8; ++rt)
                    #pragma unroll
                    for (int i = 0; i < 4; ++i) { mfC(ag[rt][i], a[rt], bg[i]); mfC(au[rt][i], a[rt], bu[i]); }
                __builtin_amdgcn_sched_barrier(0);
            }
        }
        asm volatile("s_nop 7\n\ts_nop 2" ::: "memory");
        #pragma unroll
        for (int rt = 0; rt < 8; ++rt)
            #pragma unroll
            for (int r = 0; r < 4; ++r) {
                float hv[4];
                #pragma unroll
                for (int i = 0; i < 4; ++i) {
                    if (r == 0) asm volatile("" : "+a"(ag[rt][i]), "+a"(au[rt][i]));
                    hv[i] = rbf(gelu_t(rbf(ag[rt][i][r]))) * rbf(au[rt][i][r]);
                }
                __builtin_amdgcn_raw_buffer_store_b64(u2{pk(hv[0], hv[1]), pk(hv[2], hv[3])}, c.rw, hpark, ct * 65536 + (rt * 4 + r) * 2048, 0);
                if (r == 3) __builtin_amdgcn_sched_barrier(0);
            }
    }
    drain();
    STAMP(100 + f, 0, 1, tid == 0);
    {
        const unsigned wo = ((768 * u + 8 * g) * DM + 144 * wave + 8 * cc) * 2, wo9 = ((768 * u + 8 * g) * DM + 144 * wave + 128 + cc) * 2;
        f4 acc[8][8] = {}, a9[8] = {};
        u4 rd[2][8]; unsigned rs[2][8];
        #pragma unroll
        for (int z = 0; z < 2; ++z)
            #pragma unroll
            for (int e = 0; e < 8; ++e) {
                rd[z][e] = __builtin_amdgcn_raw_buffer_load_b128(rd_, wo, (32 * z + e) * DM * 2, 0);
                rs[z][e] = __builtin_amdgcn_raw_buffer_load_b16(rd_, wo9, (32 * z + e) * DM * 2, 0);
            }
        #pragma unroll 1
        for (int k2 = 0; k2 < 24; k2 += 2) {
            if ((k2 & 7) == 0) {
                u2 hr[32];
                #pragma unroll
                for (int i = 0; i < 32; ++i) hr[i] = __builtin_amdgcn_raw_buffer_load_b64(c.rw, hpark, (k2 >> 3) * 65536 + i * 2048, 0);
                bar();
                #pragma unroll
                for (int i = 0; i < 32; ++i) *(LAS u2*)(c.sm + ((i >> 2) * 16 + g * 4 + (i & 3)) * 528 + (64 * wave + 4 * cc) * 2) = hr[i];
                bar();
            }
            #pragma unroll
            for (int z = 0; z < 2; ++z) {
                const int ks = k2 + z;
                u4 b[9];
                #pragma unroll
                for (int d = 0; d < 4; ++d) {
                    #pragma unroll
                    for (int q = 0; q < 4; ++q) { b[2 * q][d] = perm_lo(rd[z][2 * d][q], rd[z][2 * d + 1][q]); b[2 * q + 1][d] = perm_hi(rd[z][2 * d][q], rd[z][2 * d + 1][q]); }
                    b[8][d] = perm_lo(rs[z][2 * d], rs[z][2 * d + 1]);
                }
                u4 a[8];
                #pragma unroll
                for (int rt = 0; rt < 8; ++rt) a[rt] = lds16(c.sm + (rt * 16 + cc) * 528 + (ks & 7) * 64 + g * 16);
                __builtin_amdgcn_sched_barrier(0);
                const int kn = ks < 22 ? ks + 2 : ks;
                #pragma unroll
                for (int e = 0; e < 8; ++e) {
                    rd[z][e] = __builtin_amdgcn_raw_buffer_load_b128(rd_, wo, (32 * kn + e) * DM * 2, 0);
                    rs[z][e] = __builtin_amdgcn_raw_buffer_load_b16(rd_, wo9, (32 * kn + e) * DM * 2, 0);
                }
                __builtin_amdgcn_sched_barrier(0);
                #pragma unroll
                for (int rt = 0; rt < 8; ++rt) {
                    #pragma unroll
                    for (int i = 0; i < 8; ++i) mfC(acc[rt][i], a[rt], b[i]);
                    mfV(a9[rt], a[rt], b[8]);
                }
                __builtin_amdgcn_sched_barrier(0);
            }
        }
        asm volatile("s_nop 7\n\ts_nop 2" ::: "memory");
        const unsigned pb = OFF_PART + (unsigned)(p * 6 + u) * (128 * DM * 4) + (g * 4 * DM + 144 * wave) * 4;
        #pragma unroll
        for (int rt = 0; rt < 8; ++rt) {
            #pragma unroll
            for (int i = 0; i < 8; ++i) asm volatile("" : "+a"(acc[rt][i]));
            asm volatile("" : "+v"(a9[rt]));
            #pragma unroll
            for (int r = 0; r < 4; ++r) {
                const unsigned o = pb + (rt * 16 + r) * DM * 4;
                st16c(c.rw, o + cc * 32, __builtin_bit_cast(u4, f4{acc[rt][0][r], acc[rt][1][r], acc[rt][2][r], acc[rt][3][r]}));
                st16c(c.rw, o + cc * 32 + 16, __builtin_bit_cast(u4, f4{acc[rt][4][r], acc[rt][5][r], acc[rt][6][r], acc[rt][7][r]}));
                st4c(c.rw, o + 512 + cc * 4, __float_as_uint(a9[rt][r]));
            }
            __builtin_amdgcn_sched_barrier(0);
        }
    }
    drain();
    bar();
    STAMP(100 + f, 0, 2, tid == 0);
    GAS unsigned* cnt = (GAS unsigned*)(c.ws + OFF_CTL + 1024 + p * 128);
    if (tid == 0) __hip_atomic_fetch_add(cnt, 1u, __ATOMIC_RELAXED, __HIP_MEMORY_SCOPE_AGENT);
    if (wave == 0) {
        unsigned spins = 0;
        while (__hip_atomic_load(cnt, __ATOMIC_RELAXED, __HIP_MEMORY_SCOPE_AGENT) < 6u) { __builtin_amdgcn_s_sleep(8); if (++spins > 4 * SPIN_CAP) break; }
    }
    bar();
    STAMP(100 + f, 0, 3, tid == 0);
    #pragma unroll 1
    for (int it = 0; it < 12; ++it) {
        const int ci = it * 256 + tid, row = ci / 24, col = 96 * u + (ci % 24) * 4;
        const unsigned o = OFF_PART + (unsigned)(p * 6) * (128 * DM * 4) + (row * DM + col) * 4;
        u4 v[6];
        #pragma unroll
        for (int k = 0; k < 6; ++k) v[k] = ld16c(c.rw, o + k * (128 * DM * 4));
        f4 sum = __builtin_bit_cast(f4, v[0]);
        #pragma unroll
        for (int k = 1; k < 6; ++k) sum += __builtin_bit_cast(f4, v[k]);
        const u2 bv = {pk(sum[0], sum[1]), pk(sum[2], sum[3])};
        const int grow = p * 128 + row;
        const unsigned abo = OFF_AB + (grow * DM + col) * 2;
        if (grow >= P0_ROWS) st8c(c.rw, abo, u2{sane(bv.x), sane(bv.y)});
        else {
            const u2 xv = *(const u2*)(x + grow * DM + col);
            u2 av; unsigned spins = c.dead ? 8 * SPIN_CAP : 0;
            for (;;) { av = ld8c(c.rw, abo); if (__all(full2(av))) break; __builtin_amdgcn_s_sleep(32); if (++spins > 8 * SPIN_CAP) { c.dead = true; break; } }
            const unsigned t0 = pk(bflo(xv.x) + bflo(av.x), bfhi(xv.x) + bfhi(av.x)), t1 = pk(bflo(xv.y) + bflo(av.y), bfhi(xv.y) + bfhi(av.y));
            *(u2*)(out + grow * DM + col) = u2{pk(bflo(t0) + bflo(bv.x), bfhi(t0) + bfhi(bv.x)), pk(bflo(t1) + bflo(bv.y), bfhi(t1) + bfhi(bv.y))};
        }
    }
    STAMP(100 + f, 0, 4, tid == 0);
}

__global__ __launch_bounds__(256, 1) void k_main(const unsigned short* __restrict__ x, const unsigned short* __restrict__ Wq, const unsigned short* __restrict__ Wk,
                                                 const unsigned short* __restrict__ Wv, const unsigned short* __restrict__ Wo, const unsigned short* __restrict__ Wg,
                                                 const unsigned short* __restrict__ Wu, const unsigned short* __restrict__ Wd, unsigned short* __restrict__ out, char* ws) {
    extern __shared__ __attribute__((aligned(16))) char smem[];
    Ctx c;
    c.rw = __builtin_amdgcn_make_buffer_rsrc(ws, 0, WS_SPAN, 0x00020000);
    c.ws = ws; c.sm = smem; c.lane = threadIdx.x & 63; c.wave = __builtin_amdgcn_readfirstlane(threadIdx.x >> 6); c.dead = false;
    const unsigned* pw = (const unsigned*)(ws + OFF_CTL);
    c.par0 = __builtin_amdgcn_readfirstlane(pw[0]) & 1; c.par1 = __builtin_amdgcn_readfirstlane(pw[1]) & 1; c.par2 = __builtin_amdgcn_readfirstlane(pw[2]) & 1;
    const int b = blockIdx.x, r8 = b & 7, idx = (b >> 3) * 4 + (r8 & 3);
    if (r8 < 4 && idx < N_CHAIN) {
        if (idx < N_QK) role_qk(c, idx, Wq, Wk);
        else if (idx < N_QK + N_VM) role_vm(c, idx - N_QK, Wv, Wo);
        else role_red(c, idx - N_QK - N_VM);
    } else if (r8 < 4) {
        if (idx < N_CHAIN + N_QX) role_qx(c, idx - N_CHAIN, Wq);
        else if (idx < N_CHAIN + N_QX + 18) role_zx(c, idx - N_CHAIN - N_QX, Wv, Wo);
    } else {
        if (idx < N_OUT) role_out(c, idx, x, out);
        else if (idx < 32) role_zx(c, idx, Wv, Wo);
        else role_ffn(c, (r8 & 3) * 24 + (b >> 3) - 8, x, Wg, Wu, Wd, out);
    }
}

extern "C" void kernel_launch(void* const* d_in, const int* in_sizes, int n_in, void* d_out, int out_size, void* d_ws, size_t ws_size, hipStream_t stream) {
    const unsigned short* x = (const unsigned short*)d_in[0];
    char* ws = (char*)(((uintptr_t)d_ws + MB2 - 1) & ~(uintptr_t)(MB2 - 1));
    (void)hipFuncSetAttribute((const void*)k_main, hipFuncAttributeMaxDynamicSharedMemorySize, LDS_BYTES);
    k_pro<<<256, 256, 0, stream>>>(x, (const unsigned short*)d_in[1], ws);
    k_main<<<256, 256, LDS_BYTES, stream>>>(x, (const unsigned short*)d_in[2], (const unsigned short*)d_in[3], (const unsigned short*)d_in[4],
                                            (const unsigned short*)d_in[5], (const unsigned short*)d_in[8], (const unsigned short*)d_in[9],
                                            (const unsigned short*)d_in[10], (unsigned short*)d_out, ws);
}
```

```cpp
#include <hip/hip_runtime.h>
#include <hip/hip_bf16.h>
#include <stdint.h>

typedef __bf16 bf8 __attribute__((ext_vector_type(8)));
typedef __bf16 bf16x16 __attribute__((ext_vector_type(16)));
typedef __bf16 bfx2 __attribute__((ext_vector_type(2)));
typedef short s4v __attribute__((ext_vector_type(4)));
typedef float f4 __attribute__((ext_vector_type(4)));
typedef float f2 __attribute__((ext_vector_type(2)));
typedef unsigned u4 __attribute__((ext_vector_type(4)));
typedef unsigned u2 __attribute__((ext_vector_type(2)));
typedef unsigned u8v __attribute__((ext_vector_type(8)));
#define LAS __attribute__((address_space(3)))
#define GAS __attribute__((address_space(1)))
#define DEV __device__ __forceinline__

constexpr int S_TOK = 2048, DM = 576, NHEAD = 32, DH = 128, NSEG = 64, FF = 4608;
constexpr int N_QK = 32, N_VM = 32, N_RED = 18, N_CHAIN = 82, N_QX = 16, N_OUT = 24, N_ZX = 32, N_FFN = 96;
constexpr int P0_ROWS = 1792;
constexpr unsigned SPIN_CAP = 1u << 17;
constexpr unsigned MB2 = 2u << 20;
constexpr unsigned OFF_MEM = 0;
constexpr unsigned LOG_FLAGS = 640u << 10;
constexpr unsigned OFF_PM = MB2;
constexpr unsigned OFF_AB = 3 * MB2;
constexpr unsigned AB_BYTES = S_TOK * DM * 2;
constexpr unsigned LOG_ZM = 704u << 10;
constexpr unsigned ZM_SLOT = 36 * 8192;
constexpr unsigned OFF_CTL = OFF_AB + AB_BYTES;
constexpr unsigned FILL_END = OFF_CTL;
constexpr unsigned OFF_XN = 5 * MB2;
constexpr unsigned PX_SLOT = 32 * 2 * 1280;
constexpr unsigned OFF_PART = 8 * MB2;
constexpr unsigned OFF_HP = 22 * MB2;
constexpr unsigned OFF_Z = 31 * MB2;
constexpr unsigned Z_SLOT = 36 * 32768;
constexpr unsigned OFF_ZH = 68 * MB2;
constexpr unsigned OFF_PX = 78 * MB2;
constexpr unsigned OFF_QX = 81 * MB2;
constexpr unsigned WS_SPAN = OFF_QX + 64u * 32 * 8192;
constexpr unsigned FL_ZX = LOG_FLAGS, FL_ZM = LOG_FLAGS + 1024, FL_PX = LOG_FLAGS + 2048, FL_QX = LOG_FLAGS + 3072;
constexpr int LDS_BYTES = 160 * 1024;

DEV unsigned nearmap(unsigned o, unsigned par) { return ((o >> 13) << 14) + (par << 13) + (o & 8191u); }
DEV unsigned pk(float a, float b) { f2 v = {a, b}; return __builtin_bit_cast(unsigned, __builtin_convertvector(v, bfx2)); }
DEV float bflo(unsigned u) { return __uint_as_float(u << 16); }
DEV float bfhi(unsigned u) { return __uint_as_float(u & 0xffff0000u); }
DEV float rbf(float a) { return bflo(pk(a, a)); }
DEV unsigned perm_lo(unsigned a, unsigned b) { return __builtin_amdgcn_perm(b, a, 0x05040100u); }
DEV unsigned perm_hi(unsigned a, unsigned b) { return __builtin_amdgcn_perm(b, a, 0x07060302u); }
DEV f4 mfma(u4 a, u4 b, f4 c) { return __builtin_amdgcn_mfma_f32_16x16x32_bf16(__builtin_bit_cast(bf8, a), __builtin_bit_cast(bf8, b), c, 0, 0, 0); }
DEV f4 smf(u4 a, u8v b, f4 c, int idx) { return __builtin_amdgcn_smfmac_f32_16x16x64_bf16(__builtin_bit_cast(bf8, a), __builtin_bit_cast(bf16x16, b), c, idx, 0, 0); }
DEV u4 lo4(u8v b) { return __builtin_shufflevector(b, b, 0, 1, 2, 3); }
DEV u4 hi4(u8v b) { return __builtin_shufflevector(b, b, 4, 5, 6, 7); }
DEV u8v cat8(u4 a, u4 b) { return __builtin_shufflevector(a, b, 0, 1, 2, 3, 4, 5, 6, 7); }
DEV float fold32(float a, float b) {
    auto r = __builtin_amdgcn_permlane32_swap(__float_as_uint(a), __float_as_uint(b), false, false);
    return __uint_as_float(r[0]) + __uint_as_float(r[1]);
}
DEV float xmax32(float v) { auto r = __builtin_amdgcn_permlane32_swap(__float_as_uint(v), __float_as_uint(v), false, false); return fmaxf(__uint_as_float(r[0]), __uint_as_float(r[1])); }
DEV float xsum32(float v) { auto r = __builtin_amdgcn_permlane32_swap(__float_as_uint(v), __float_as_uint(v), false, false); return __uint_as_float(r[0]) + __uint_as_float(r[1]); }
DEV float xmax16(float v) { auto r = __builtin_amdgcn_permlane16_swap(__float_as_uint(v), __float_as_uint(v), false, false); return fmaxf(__uint_as_float(r[0]), __uint_as_float(r[1])); }
DEV float xsum16(float v) { auto r = __builtin_amdgcn_permlane16_swap(__float_as_uint(v), __float_as_uint(v), false, false); return __uint_as_float(r[0]) + __uint_as_float(r[1]); }
DEV float ror8(float v) { return __uint_as_float(__builtin_amdgcn_update_dpp(0u, __float_as_uint(v), 0x128, 0xf, 0xf, false)); }

typedef __amdgpu_buffer_rsrc_t rsrc_t;
DEV u4 ld16c(rsrc_t r, unsigned off) { asm volatile("" ::: "memory"); return __builtin_amdgcn_raw_buffer_load_b128(r, off, 0, 16); }
DEV u2 ld8c(rsrc_t r, unsigned off) { asm volatile("" ::: "memory"); return __builtin_amdgcn_raw_buffer_load_b64(r, off, 0, 16); }
DEV unsigned ld4c(rsrc_t r, unsigned off) { asm volatile("" ::: "memory"); return __builtin_amdgcn_raw_buffer_load_b32(r, off, 0, 16); }
DEV void st16c(rsrc_t r, unsigned off, u4 v) { __builtin_amdgcn_raw_buffer_store_b128(v, r, off, 0, 16); }
DEV void st8c(rsrc_t r, unsigned off, u2 v) { __builtin_amdgcn_raw_buffer_store_b64(v, r, off, 0, 16); }
DEV void st4c(rsrc_t r, unsigned off, unsigned v) { __builtin_amdgcn_raw_buffer_store_b32(v, r, off, 0, 16); }
DEV void drain() { asm volatile("s_waitcnt vmcnt(0)" ::: "memory"); }
DEV void bar() { asm volatile("s_waitcnt lgkmcnt(0)\n\ts_barrier" ::: "memory"); }
DEV bool full4(u4 v) { return (v.x != ~0u) & (v.y != ~0u) & (v.z != ~0u) & (v.w != ~0u); }
DEV bool full2(u2 v) { return (v.x != ~0u) & (v.y != ~0u); }
DEV unsigned sane(unsigned v) { return v == ~0u ? 0x7fff7fffu : v; }
DEV u4 lds16(const char* p) { return *(const LAS u4*)(p); }
DEV int aperm(int g, int e) { return 16 * (g >> 1) + 4 * (g & 1) + 8 * ((e >> 1) & 1) + 2 * (e >> 2) + (e & 1); }

__global__ __launch_bounds__(256) void k_pro(const unsigned short* __restrict__ x, const unsigned short* __restrict__ mem0, char* __restrict__ ws) {
    const int tid = threadIdx.x, lane = tid & 63, wave = tid >> 6, b = blockIdx.x;
    __shared__ __attribute__((aligned(16))) unsigned short rowbuf[4][2][DM];
    if (b == 256) {
      if (lane == 0 && wave < 3) {
        unsigned lat[2] = {~0u, ~0u};
        for (int it = 0; it < 10; ++it) {
            const int par = it & 1;
            GAS unsigned* p = (GAS unsigned*)(ws + wave * MB2 + MB2 - 16384 + par * 8192);
            unsigned xv, zero = 0;
            const unsigned long long c0 = __builtin_amdgcn_s_memtime();
            asm volatile("global_atomic_add %0, %1, %2, off sc0\n\ts_waitcnt vmcnt(0)" : "=&v"(xv) : "v"(p), "v"(zero) : "memory");
            const unsigned d = (unsigned)(__builtin_amdgcn_s_memtime() - c0);
            lat[par] = d < lat[par] ? d : lat[par];
        }
        *(unsigned*)(ws + OFF_CTL + wave * 4) = lat[1] < lat[0] ? 1u : 0u;
    }
      return;
    }
    {
        const u4 ones = {~0u, ~0u, ~0u, ~0u}, zero = {0, 0, 0, 0};
        for (unsigned o = (b * 256 + tid) * 16; o < FILL_END; o += 256 * 256 * 16) {
            if (o < MB2) {
                unsigned lg = ((o >> 14) << 13) + (o & 8191u);
                if (lg < 9216) continue;
                if (lg >= LOG_FLAGS + 4096) continue;
                *(u4*)(ws + o) = (lg >= LOG_FLAGS) ? zero : ones;
            } else if (o < 3 * MB2) {
                const unsigned q = o & (MB2 - 1);
                if ((q >= 2 * 32 * 20480 && q < 2 * LOG_ZM) || q >= 2 * (LOG_ZM + ZM_SLOT)) continue;
                *(u4*)(ws + o) = ones;
            } else *(u4*)(ws + o) = ones;
        }
    }
    if (b == 1) {
        for (int i = tid; i < 576; i += 256) {
            int t = i >> 6, l = i & 63, r = l & 7;
            u4 v = *(const u4*)(mem0 + r * DM + 64 * t + 8 * (l >> 3));
            v.x = sane(v.x); v.y = sane(v.y); v.z = sane(v.z); v.w = sane(v.w);
            *(u4*)(ws + OFF_MEM + nearmap(i * 16, 0)) = v;
            *(u4*)(ws + OFF_MEM + nearmap(i * 16, 1)) = v;
        }
        if (tid < 192) *(u4*)(ws + OFF_CTL + 1024 + tid * 16) = u4{0, 0, 0, 0};
    }
    #pragma unroll
    for (int rr = 0; rr < 2; ++rr) {
        const int row = b * 8 + wave * 2 + rr;
        float v[9]; float s = 0.f;
        #pragma unroll
        for (int i = 0; i < 9; ++i) { v[i] = __uint_as_float((unsigned)x[row * DM + lane + 64 * i] << 16); s += v[i]; }
        #pragma unroll
        for (int o = 32; o; o >>= 1) s += __shfl_xor(s, o);
        const float mean = s * (1.f / DM);
        float q = 0.f;
        #pragma unroll
        for (int i = 0; i < 9; ++i) { float d = v[i] - mean; q += d * d; }
        #pragma unroll
        for (int o = 32; o; o >>= 1) q += __shfl_xor(q, o);
        const float mb = rbf(mean), vb = rbf(q * (1.f / DM));
        const float sd = rbf(sqrtf(rbf(vb + 1e-5f)));
        #pragma unroll
        for (int i = 0; i < 9; ++i) rowbuf[wave][rr][lane + 64 * i] = (unsigned short)(pk(rbf(v[i] - mb) / sd, 0.f) & 0xffffu);
    }
    __syncthreads();
    for (int i = tid; i < 8 * 72; i += 256) {
        const int lr = i / 72, ch = i % 72, ks = ch >> 2, g = ch & 3, row = b * 8 + lr;
        const unsigned short* rb = rowbuf[lr >> 1][lr & 1] + 32 * ks + 16 * (g >> 1) + 4 * (g & 1);
        u2 p0 = *(const u2*)rb, p1 = *(const u2*)(rb + 8);
        u4 o = {p0.x, p1.x, p0.y, p1.y};
        *(u4*)(ws + OFF_XN + ((row >> 4) * 18 + ks) * 1024 + (g * 16 + (row & 15)) * 16) = o;
    }
}
struct Ctx { rsrc_t rw; const char* ws; char* sm; unsigned par0, par1, par2; int lane, wave; bool dead; };

DEV void stage_mem(Ctx& c, int s, char* mems) {
    const unsigned lb = s * 9216 + c.lane * 16;
    const unsigned o0 = OFF_MEM + nearmap(lb + c.wave * 1024, c.par0), o1 = OFF_MEM + nearmap(lb + (c.wave + 4) * 1024, c.par0);
    const unsigned o2 = OFF_MEM + nearmap(lb + 8 * 1024, c.par0);
    u4 x0, x1, x2;
    unsigned spins = c.dead ? SPIN_CAP : 0;
    if (c.wave == 3) {
        for (;;) {
            x0 = ld16c(c.rw, o0); x1 = ld16c(c.rw, o1); x2 = ld16c(c.rw, o2);
            if (__all(full4(x0) & full4(x1) & full4(x2))) break;
            if (++spins > SPIN_CAP) { c.dead = true; break; }
        }
        *(LAS u4*)(mems + 8192 + c.lane * 16) = x2;
    } else {
        for (;;) {
            x0 = ld16c(c.rw, o0); x1 = ld16c(c.rw, o1);
            if (__all(full4(x0) & full4(x1))) break;
            if (++spins > SPIN_CAP) { c.dead = true; break; }
        }
    }
    *(LAS u4*)(mems + c.wave * 1024 + c.lane * 16) = x0;
    *(LAS u4*)(mems + (c.wave + 4) * 1024 + c.lane * 16) = x1;
    bar();
}
DEV void wait_flags(Ctx& c, unsigned off, unsigned want) {
    unsigned spins = c.dead ? SPIN_CAP : 0;
    for (;;) {
        unsigned v = ld4c(c.rw, off);
        if (__all(v >= want)) break;
        __builtin_amdgcn_s_sleep(6);
        if (++spins > SPIN_CAP) { c.dead = true; break; }
    }
}
DEV void xn_load(const Ctx& c, int seg, u4 (&xr)[9]) {
    const u4* p = (const u4*)(c.ws + OFF_XN + seg * 36864 + c.wave * 9216) + c.lane;
    #pragma unroll
    for (int i = 0; i < 9; ++i) xr[i] = p[i * 64];
}
DEV void xn_put(const Ctx& c, char* xnb, const u4 (&xr)[9]) {
    #pragma unroll
    for (int i = 0; i < 9; ++i) *(LAS u4*)(xnb + c.wave * 9216 + i * 1024 + c.lane * 16) = xr[i];
}
DEV void xn_dma(const Ctx& c, int seg, unsigned lds_off) {
    const char* src = c.ws + OFF_XN + seg * 36864 + c.wave * 9216 + c.lane * 16;
    const unsigned dst = (unsigned)(uintptr_t)(LAS char*)c.sm + lds_off + c.wave * 9216;
    #pragma unroll
    for (int i = 0; i < 9; ++i) {
        unsigned keep;
        asm volatile("s_mov_b32 %0, m0\n\ts_mov_b32 m0, %2\n\ts_nop 0\n\tglobal_load_lds_dwordx4 %1, off\n\ts_mov_b32 m0, %0"
                     : "=&s"(keep) : "v"(src + i * 1024), "s"(dst + i * 1024) : "memory");
    }
}
constexpr float SC_L2E = 0.08838834764831845f * 1.4426950408889634f;
DEV void smfA(f4& c, u4 a, const u8v& w, int idx) { asm volatile("v_smfmac_f32_16x16x64_bf16 %0, %1, %2, %3" : "+v"(c) : "v"(a), "a"(w), "v"(idx)); }
DEV void smfV(f4& c, u4 a, const u8v& w, int idx) { asm volatile("v_smfmac_f32_16x16x64_bf16 %0, %1, %2, %3" : "+v"(c) : "v"(a), "v"(w), "v"(idx)); }
DEV void mfA(f4& c, u4 a, u4 w) { asm volatile("v_mfma_f32_16x16x32_bf16 %0, %1, %2, %0" : "+v"(c) : "v"(a), "a"(w)); }
DEV void mfV(f4& c, u4 a, u4 w) { asm volatile("v_mfma_f32_16x16x32_bf16 %0, %1, %2, %0" : "+v"(c) : "v"(a), "v"(w)); }
DEV void pre2(f4& a, f4& b) { asm volatile("s_nop 1" : "+v"(a), "+v"(b)); }
DEV void post2(f4& a, f4& b) { asm volatile("s_nop 7\n\ts_nop 2" : "+v"(a), "+v"(b)); }
DEV void pin2(f4& a, f4& b) { asm volatile("" : "+v"(a), "+v"(b)); }

DEV void fetch36(const Ctx& c, const unsigned short* __restrict__ src, int stride, u4 (&r)[36]) {
    #pragma unroll
    for (int i = 0; i < 36; ++i) r[i] = *(const u4*)(src + i * stride);
    __builtin_amdgcn_sched_barrier(0);
}
DEV const unsigned short* proj_src(const Ctx& c, const unsigned short* W) { const int tid = c.wave * 64 + c.lane; return W + (tid >> 4) * 4096 + (tid & 15) * 8; }
DEV void load_proj(const Ctx& c, u4 (&r)[36], const unsigned short* __restrict__ nsrc, int nstride, u8v (&w0)[9], u8v (&w1)[9]) {
    const int tid = c.wave * 64 + c.lane, cc = c.lane & 15, g = c.lane >> 4;
    bar();
    #pragma unroll
    for (int i = 0; i < 36; ++i) {
        *(LAS u4*)(c.sm + (i * 16 + (tid >> 4)) * 272 + (tid & 15) * 16) = r[i];
        if (nsrc) r[i] = *(const u4*)(nsrc + i * nstride);
    }
    __builtin_amdgcn_sched_barrier(0);
    bar();
    const int q = cc >> 2, p = cc & 3;
    const char* tb = c.sm + (16 * (g >> 1) + 4 * (g & 1) + (q & 1) + 8 * (q >> 1)) * 272 + (c.wave * 32 + 4 * p) * 2;
    #pragma unroll
    for (int t = 0; t < 9; ++t)
        #pragma unroll
        for (int k = 0; k < 4; ++k) {
            const char* a = tb + (64 * t + 32 * (k >> 1) + 2 * (k & 1)) * 272;
            const u2 v0 = __builtin_bit_cast(u2, __builtin_amdgcn_ds_read_tr16_b64_v4i16((LAS s4v*)a));
            const u2 v1 = __builtin_bit_cast(u2, __builtin_amdgcn_ds_read_tr16_b64_v4i16((LAS s4v*)(a + 32)));
            w0[t][2 * k] = v0.x; w0[t][2 * k + 1] = v0.y; w1[t][2 * k] = v1.x; w1[t][2 * k + 1] = v1.y;
        }
}

DEV void role_qk(Ctx& c, int h, const unsigned short* __restrict__ Wq, const unsigned short* __restrict__ Wk) {
    const int lane = c.lane, wave = c.wave, cc = lane & 15, g = lane >> 4;
    char* const MEMS = c.sm + 73728; char* const QM = c.sm + 82944; char* const KM = QM + 2176;
    char* const KX = KM + 2176;
    const int sidx = (lane & 8) ? 0xEEEE : 0x4444;
    u8v q0[9], q1[9], k0[9], k1[9];
    {
        u4 r[36];
        fetch36(c, proj_src(c, Wq + h * DH), 16 * 4096, r);
        load_proj(c, r, proj_src(c, Wk + h * DH), 16 * 4096, q0, q1);
        load_proj(c, r, nullptr, 0, k0, k1);
    }
    const int xw = (wave * 16 + cc) * 4 + g * 4 * 272;
    const int mw = (wave * 16 + cc) * 4 + ((g & 1) * 4 + (lane >> 5) * 2) * 272;
    const unsigned pxflag = OFF_MEM + nearmap(FL_PX + (h * 2 + (wave - 1)) * 4, c.par0);
    bar();
    xn_dma(c, 0, 0);
    drain();
    bar();
    u4 q[4] = {};
    unsigned qflag = 0;
    const unsigned fqo = OFF_MEM + nearmap(FL_QX + (h * 2 + ((wave - 1) & 1)) * 4, c.par0);
    for (int s = -1; s < NSEG; ++s) {
        if (s >= 0) {
            u4 kf[8];
            if (wave < 3) {
                #pragma unroll
                for (int i = 0; i < 8; ++i) kf[i] = lds16(KX + ((i >> 2) * 16 + cc) * 272 + (i & 3) * 64 + g * 16);
            }
            const unsigned pb = OFF_PM + (s >> 5) * MB2, par = (s >> 5) ? c.par2 : c.par1, lb = (s & 31) * 20480;
            unsigned po0 = pb + nearmap(lb + h * 512 + ((g >> 1) * 16 + cc) * 16 + (g & 1) * 8, par);
            unsigned po1 = pb + nearmap(lb + 16384 + h * 128 + cc * 8 + (g & 1) * 4, par);
            f4 aq0 = {}, aq1 = {}, ak0 = {}, ak1 = {};
            pin2(aq0, aq1); pin2(ak0, ak1);
            asm volatile("" : "+v"(po0), "+v"(po1));
            stage_mem(c, s, MEMS);
            u4 a[9];
            #pragma unroll
            for (int t = 0; t < 9; ++t) a[t] = lds16(MEMS + t * 1024 + lane * 16);
            #pragma unroll
            for (int t = 0; t < 9; ++t) {
                smfA(aq0, a[t], q0[t], sidx); smfA(aq1, a[t], q1[t], sidx);
                if (t < 7) { smfA(ak0, a[t], k0[t], sidx); smfA(ak1, a[t], k1[t], sidx); }
                else { smfV(ak0, a[t], k0[t], sidx); smfV(ak1, a[t], k1[t], sidx); }
            }
            post2(aq0, aq1); pin2(ak0, ak1);
            #pragma unroll
            for (int r = 0; r < 2; ++r) {
                *(LAS unsigned*)(QM + mw + r * 272) = pk(fold32(aq0[r], aq0[r + 2]), fold32(aq1[r], aq1[r + 2]));
                *(LAS unsigned*)(KM + mw + r * 272) = pk(fold32(ak0[r], ak0[r + 2]), fold32(ak1[r], ak1[r + 2]));
            }
            bar();
            if (wave < 3 && (wave > 0 || s < NSEG - 1)) {
                f4 s0 = {}, s1 = {}, sm = {};
                u4 km[4];
                if (wave == 0) {
                    #pragma unroll
                    for (int ks = 0; ks < 4; ++ks) q[ks] = lds16(QM + (lane & 7) * 272 + ks * 64 + g * 16);
                }
                #pragma unroll
                for (int ks = 0; ks < 4; ++ks) km[ks] = lds16(KM + (lane & 7) * 272 + ks * 64 + g * 16);
                #pragma unroll
                for (int ks = 0; ks < 4; ++ks) { s0 = mfma(kf[ks], q[ks], s0); s1 = mfma(kf[4 + ks], q[ks], s1); sm = mfma(km[ks], q[ks], sm); }
                const float NEG = -3.0e38f;
                if (wave == 0) {
                    const bool hh = lane & 8;
                    float v0 = hh ? s0[2] : s0[0], v1 = hh ? s0[3] : s0[1], v2 = hh ? s1[2] : s1[0], v3 = hh ? s1[3] : s1[1];
                    float v4 = hh ? sm[2] : sm[0], v5 = hh ? sm[3] : sm[1];
                    if (g >= 2) { v4 = NEG; v5 = NEG; }
                    float m = fmaxf(fmaxf(fmaxf(v0, v1), fmaxf(v2, v3)), fmaxf(v4, v5));
                    m = fmaxf(m, ror8(m)); m = xmax16(m); m = xmax32(m);
                    const float mc = m * SC_L2E;
                    v0 = __builtin_amdgcn_exp2f(v0 * SC_L2E - mc); v1 = __builtin_amdgcn_exp2f(v1 * SC_L2E - mc);
                    v2 = __builtin_amdgcn_exp2f(v2 * SC_L2E - mc); v3 = __builtin_amdgcn_exp2f(v3 * SC_L2E - mc);
                    v4 = __builtin_amdgcn_exp2f(v4 * SC_L2E - mc); v5 = __builtin_amdgcn_exp2f(v5 * SC_L2E - mc);
                    if (g >= 2) { v4 = 0.f; v5 = 0.f; }
                    float l = ((v0 + v1) + (v2 + v3)) + (v4 + v5);
                    l += ror8(l); l = xsum16(l); l = xsum32(l);
                    const float inv = __builtin_amdgcn_rcpf(l);
                    u2 o = {pk(v0 * inv, v1 * inv) & 0x7fff7fffu, pk(v2 * inv, v3 * inv) & 0x7fff7fffu};
                    st8c(c.rw, po0, o);
                    st4c(c.rw, g < 2 ? po1 : 0xfffffff0u, pk(v4 * inv, v5 * inv) & 0x7fff7fffu);
                } else {
                    const int qi = (wave - 1) * 16 + cc;
                    float v[12];
                    #pragma unroll
                    for (int r = 0; r < 4; ++r) {
                        v[r] = (4 * g + r <= qi) ? s0[r] : NEG; v[4 + r] = (16 + 4 * g + r <= qi) ? s1[r] : NEG; v[8 + r] = (g < 2) ? sm[r] : NEG;
                    }
                    float m = v[0];
                    #pragma unroll
                    for (int r = 1; r < 12; ++r) m = fmaxf(m, v[r]);
                    m = xmax16(m); m = xmax32(m);
                    const float mc = m * SC_L2E;
                    float l = 0.f;
                    #pragma unroll
                    for (int r = 0; r < 12; ++r) { float e = __builtin_amdgcn_exp2f(v[r] * SC_L2E - mc); v[r] = (v[r] == NEG) ? 0.f : e; l += v[r]; }
                    l = xsum16(l); l = xsum32(l);
                    const float inv = __builtin_amdgcn_rcpf(l);
                    const unsigned px = OFF_PX + s * PX_SLOT + (h * 2 + wave - 1) * 1280;
                    u4 o = {pk(v[0] * inv, v[1] * inv), pk(v[2] * inv, v[3] * inv), pk(v[4] * inv, v[5] * inv), pk(v[6] * inv, v[7] * inv)};
                    st16c(c.rw, px + lane * 16, o);
                    if (g < 2) st8c(c.rw, px + 1024 + cc * 16 + g * 8, u2{pk(v[8] * inv, v[9] * inv), pk(v[10] * inv, v[11] * inv)});
                }
            }
        }
        const bool pxw = (wave == 1 || wave == 2) && s >= 0;
        if (s == NSEG - 1) { if (pxw) { drain(); if (lane == 0) st4c(c.rw, pxflag, s + 1); } break; }
        const char* XNB = c.sm + ((s + 1) & 1) * 36864;
        f4 acc[2][4] = {};
        #pragma unroll
        for (int i = 2; i < 4; ++i) pre2(acc[0][i], acc[1][i]);
        u4 n0 = lds16(XNB + lane * 16), n1 = lds16(XNB + 18 * 1024 + lane * 16);
        #pragma unroll
        for (int ks = 0; ks < 18; ++ks) {
            const u4 a0 = n0, a1 = n1;
            if (ks < 17) { n0 = lds16(XNB + (ks + 1) * 1024 + lane * 16); n1 = lds16(XNB + (19 + ks) * 1024 + lane * 16); }
            const int t = ks >> 1;
            const u4 b2 = (ks & 1) ? hi4(k0[t]) : lo4(k0[t]), b3 = (ks & 1) ? hi4(k1[t]) : lo4(k1[t]);
            if (t < 7) { mfA(acc[0][2], a0, b2); mfA(acc[1][2], a1, b2); mfA(acc[0][3], a0, b3); mfA(acc[1][3], a1, b3); }
            else { mfV(acc[0][2], a0, b2); mfV(acc[1][2], a1, b2); mfV(acc[0][3], a0, b3); mfV(acc[1][3], a1, b3); }
            if (ks == 8) {
                if (pxw) { drain(); if (lane == 0) st4c(c.rw, pxflag, s + 1); }
                if (wave == 1 || wave == 2) {
                    if (!__all(qflag >= (unsigned)(s + 2))) wait_flags(c, fqo, s + 2);
                    #pragma unroll
                    for (int i = 0; i < 4; ++i) q[i] = ld16c(c.rw, OFF_QX + (((s + 1) * 32 + h) * 2 + wave - 1) * 4096 + i * 1024 + lane * 16);
                    qflag = ld4c(c.rw, fqo);
                }
                if (s + 2 < NSEG) xn_dma(c, s + 2, (s & 1) * 36864);
            }
        }
        bar();
        post2(acc[0][3], acc[1][3]);
        pin2(acc[0][2], acc[1][2]);
        #pragma unroll
        for (int rt = 0; rt < 2; ++rt)
            #pragma unroll
            for (int r = 0; r < 4; ++r) {
                *(LAS unsigned*)(KX + xw + (rt * 16 + r) * 272) = pk(acc[rt][2][r], acc[rt][3][r]);
            }
        bar();
    }
}

DEV void load_vo(const Ctx& c, int h, const unsigned short* __restrict__ Wv, const unsigned short* __restrict__ Wo, u8v (&v0)[9], u8v (&v1)[9], u8v (&wo)[2][9]) {
    const int lane = c.lane, wave = c.wave, cc = lane & 15, g = lane >> 4, tid = wave * 64 + lane;
    u4 r[36];
    fetch36(c, proj_src(c, Wv + h * DH), 16 * 4096, r);
    load_proj(c, r, Wo + (size_t)h * DH * DM + tid * 8, 2048, v0, v1);
    {
        bar();
        #pragma unroll
        for (int i = 0; i < 36; ++i) { const int ch = i * 256 + tid; *(LAS u4*)(c.sm + (ch / 72) * 1168 + (ch % 72) * 16) = r[i]; }
        bar();
        const int q = cc >> 2, p = cc & 3;
        const char* tb = c.sm + (8 * (g >> 1) + 2 * (g & 1) + 4 * (q >> 1) + 16 * (q & 1)) * 1168 + (wave * 144 + 4 * p) * 2;
        #pragma unroll
        for (int t = 0; t < 2; ++t)
            #pragma unroll
            for (int u = 0; u < 9; ++u)
                #pragma unroll
                for (int k = 0; k < 4; ++k) {
                    const u2 w2 = __builtin_bit_cast(u2, __builtin_amdgcn_ds_read_tr16_b64_v4i16((LAS s4v*)(tb + (64 * t + 32 * (k >> 1) + (k & 1)) * 1168 + u * 32)));
                    wo[t][u][2 * k] = w2.x; wo[t][u][2 * k + 1] = w2.y;
                }
    }
}
DEV void role_vm(Ctx& c, int h, const unsigned short* __restrict__ Wv, const unsigned short* __restrict__ Wo) {
    const int lane = c.lane, wave = c.wave, cc = lane & 15, g = lane >> 4, tid = wave * 64 + lane;
    char* const MEMS = c.sm; char* const VM = c.sm + 9216;
    const int sidx = (lane & 8) ? 0xEEEE : 0x4444;
    u8v v0[9], v1[9], wo[2][9];
    load_vo(c, h, Wv, Wo, v0, v1, wo);
    char* const MW = VM + (wave * 16 + cc) * 4 + ((g & 1) * 4 + (lane >> 5) * 2) * 272;
    bar();
    for (int s = 0; s < NSEG; ++s) {
            f4 a0 = {}, a1 = {}, z[9] = {};
            pin2(a0, a1);
            #pragma unroll
            for (int u = 0; u < 8; u += 2) pin2(z[u], z[u + 1]);
            pin2(z[8], z[0]);
            const unsigned zl = (wave * 9 + (lane >> 5)) * 8192 + h * 256 + cc * 16 + (g & 1) * 8;
            const unsigned zb = OFF_PM + (s & 1) * MB2 + nearmap(LOG_ZM + zl, (s & 1) ? c.par2 : c.par1);
            stage_mem(c, s, MEMS);
            u4 a[9];
            #pragma unroll
            for (int t = 0; t < 9; ++t) a[t] = lds16(MEMS + t * 1024 + lane * 16);
            #pragma unroll
            for (int t = 0; t < 9; ++t) { smfA(a0, a[t], v0[t], sidx); smfA(a1, a[t], v1[t], sidx); }
            post2(a0, a1);
            #pragma unroll
            for (int r = 0; r < 2; ++r) *(LAS unsigned*)(MW + r * 272) = pk(fold32(a0[r], a0[r + 2]), fold32(a1[r], a1[r + 2]));
            bar();
            const u4 va = lds16(VM + (lane & 7) * 272 + (lane >> 3) * 16), vb = lds16(VM + (lane & 7) * 272 + 128 + (lane >> 3) * 16);
            #pragma unroll
            for (int u = 0; u < 9; ++u) { smfA(z[u], va, wo[0][u], sidx); if (u < 5) smfA(z[u], vb, wo[1][u], sidx); else smfV(z[u], vb, wo[1][u], sidx); }
            post2(z[8], z[7]);
            u2 zo[5];
            #pragma unroll
            for (int u = 0; u < 8; u += 2) {
                pin2(z[u], z[u + 1]);
                zo[u >> 1] = u2{sane(pk(fold32(z[u][0], z[u + 1][0]), fold32(z[u][1], z[u + 1][1]))), sane(pk(fold32(z[u][2], z[u + 1][2]), fold32(z[u][3], z[u + 1][3])))};
                if (s < NSEG - 1) st8c(c.rw, zb + u * 16384, zo[u >> 1]);
            }
            zo[4] = u2{sane(pk(fold32(z[8][0], z[8][0]), fold32(z[8][1], z[8][1]))), sane(pk(fold32(z[8][2], z[8][2]), fold32(z[8][3], z[8][3])))};
            if (lane < 32 && s < NSEG - 1) st8c(c.rw, zb + 8 * 16384, zo[4]);
            const unsigned zh = OFF_ZH + s * ZM_SLOT + zl;
            #pragma unroll
            for (int u = 0; u < 4; ++u) st8c(c.rw, zh + u * 16384, zo[u]);
            if (lane < 32) st8c(c.rw, zh + 8 * 8192, zo[4]);
            drain();
            if (lane == 0) st4c(c.rw, OFF_MEM + nearmap(FL_ZM + (wave * 32 + h) * 4, c.par0), s + 1);
    }
}
DEV void role_zx(Ctx& c, int h, const unsigned short* __restrict__ Wv, const unsigned short* __restrict__ Wo) {
    const int lane = c.lane, wave = c.wave, cc = lane & 15, g = lane >> 4, tid = wave * 64 + lane;
    char* const XNB = c.sm; char* const VX = c.sm + 36864;
    u8v v0[9], v1[9], wo[2][9];
    load_vo(c, h, Wv, Wo, v0, v1, wo);
    u4 xr[9];
    xn_load(c, 0, xr);
    bar();
    xn_put(c, XNB, xr);
    xn_load(c, 1, xr);
    char* const XW = VX + (wave * 16 + 8 * (cc >> 3) + 4 * ((cc >> 1) & 1) + ((cc >> 2) & 1) + 2 * (cc & 1)) * 4 + g * 4 * 272;
    const unsigned fzx = OFF_MEM + nearmap(FL_ZX + (wave * 32 + h) * 4, c.par0);
    bar();
    for (int s = 0; s < NSEG; ++s) {
        f4 acc[2][2] = {};
        pre2(acc[0][0], acc[1][0]); pre2(acc[0][1], acc[1][1]);
        u4 n0 = lds16(XNB + lane * 16), n1 = lds16(XNB + 18 * 1024 + lane * 16);
        #pragma unroll
        for (int ks = 0; ks < 18; ++ks) {
            const u4 x0 = n0, x1 = n1;
            if (ks < 17) { n0 = lds16(XNB + (ks + 1) * 1024 + lane * 16); n1 = lds16(XNB + (19 + ks) * 1024 + lane * 16); }
            const u4 b0 = (ks & 1) ? hi4(v0[ks >> 1]) : lo4(v0[ks >> 1]), b1 = (ks & 1) ? hi4(v1[ks >> 1]) : lo4(v1[ks >> 1]);
            mfA(acc[0][0], x0, b0); mfA(acc[1][0], x1, b0); mfA(acc[0][1], x0, b1); mfA(acc[1][1], x1, b1);
        }
        if (s > 0) { drain(); if (lane == 0) st4c(c.rw, fzx, s); }
        bar();
        post2(acc[0][1], acc[1][1]); pin2(acc[0][0], acc[1][0]);
        #pragma unroll
        for (int rt = 0; rt < 2; ++rt)
            #pragma unroll
            for (int r = 0; r < 4; ++r) *(LAS unsigned*)(XW + (rt * 16 + r) * 272) = pk(acc[rt][0][r], acc[rt][1][r]);
        if (s + 1 < NSEG) xn_put(c, XNB, xr);
        bar();
        if (s + 2 < NSEG) xn_load(c, s + 2, xr);
        u4 vf[8];
        #pragma unroll
        for (int i = 0; i < 8; ++i) vf[i] = lds16(VX + ((i >> 2) * 16 + cc) * 272 + (i & 3) * 64 + g * 16);
        const unsigned zx = OFF_Z + s * Z_SLOT + wave * 9 * 32768 + h * 1024 + lane * 16;
        f4 z0[9] = {}, z1[9] = {};
        #pragma unroll
        for (int u = 0; u < 9; ++u) pre2(z0[u], z1[u]);
        #pragma unroll
        for (int u = 0; u < 9; ++u) {
            #pragma unroll
            for (int ks = 0; ks < 4; ++ks) {
                const u4 b = (ks & 1) ? hi4(wo[ks >> 1][u]) : lo4(wo[ks >> 1][u]);
                if (ks < 2 || u < 5) { mfA(z0[u], vf[ks], b); mfA(z1[u], vf[4 + ks], b); } else { mfV(z0[u], vf[ks], b); mfV(z1[u], vf[4 + ks], b); }
            }
            if (u > 0) {
                pin2(z0[u - 1], z1[u - 1]);
                st16c(c.rw, zx + (u - 1) * 32768, u4{pk(z0[u - 1][0], z0[u - 1][1]), pk(z0[u - 1][2], z0[u - 1][3]), pk(z1[u - 1][0], z1[u - 1][1]), pk(z1[u - 1][2], z1[u - 1][3])});
            }
        }
        post2(z0[8], z1[8]);
        st16c(c.rw, zx + 8 * 32768, u4{pk(z0[8][0], z0[8][1]), pk(z0[8][2], z0[8][3]), pk(z1[8][0], z1[8][1]), pk(z1[8][2], z1[8][3])});
        if (s == 0 || s == NSEG - 1) { drain(); if (lane == 0) st4c(c.rw, fzx, s + 1); }
    }
}

DEV void role_red(Ctx& c, int o) {
    const int lane = c.lane, wave = c.wave, cc = lane & 15, g = lane >> 4, tid = wave * 64 + lane, j0 = 2 * o;
    char* const RM = c.sm;
    const int sidx = (lane & 8) ? 0xEEEE : 0x4444;
    const unsigned fzx = OFF_MEM + nearmap(FL_ZX + (((j0 + ((lane >> 3) & 1)) / 9) * 32 + wave * 8 + (lane & 7)) * 4, c.par0);
    const unsigned zb0 = OFF_Z + j0 * 32768 + wave * 8192 + lane * 16;
    u4 zn[2][8];
    wait_flags(c, fzx, 1);
    #pragma unroll
    for (int i = 0; i < 8; ++i) { zn[0][i] = ld16c(c.rw, zb0 + i * 1024); zn[1][i] = ld16c(c.rw, zb0 + 32768 + i * 1024); }
    unsigned zflag = 0;
    for (int s = 0; s < NSEG - 1; ++s) {
        const unsigned zm = OFF_PM + (s & 1) * MB2 + nearmap(LOG_ZM + j0 * 8192 + (wave * 8 + g) * 256 + cc * 16, (s & 1) ? c.par2 : c.par1);
        const unsigned pb = OFF_PM + (s >> 5) * MB2, par = (s >> 5) ? c.par2 : c.par1, lb = (s & 31) * 20480;
        const unsigned o0 = pb + nearmap(lb + wave * 4096 + lane * 16, par);
        const unsigned o1 = pb + nearmap(lb + 16384 + (wave * 8 + 2 * g) * 128 + cc * 8, par);
        unsigned om = OFF_MEM + nearmap((s + 1) * 9216 + (j0 + (wave & 1)) * 256 + lane * 4, c.par0);
        asm volatile("" : "+v"(om));
        u4 zf[2][10];
        #pragma unroll
        for (int i = 0; i < 8; ++i) { zf[0][i] = zn[0][i]; zf[1][i] = zn[1][i]; }
        u4 p[4]; u2 m0, m1;
        #pragma unroll
        for (int i = 0; i < 4; ++i) p[i] = ld16c(c.rw, o0 + i * 1024);
        m0 = ld8c(c.rw, o1); m1 = ld8c(c.rw, o1 + 128);
        asm volatile("" :: "v"(p[0]), "v"(p[1]), "v"(p[2]), "v"(p[3]), "v"(m0), "v"(m1));
        unsigned spins = c.dead ? SPIN_CAP : 0;
        for (;;) {
            zf[0][8] = ld16c(c.rw, zm); zf[0][9] = ld16c(c.rw, zm + 1024); zf[1][8] = ld16c(c.rw, zm + 16384); zf[1][9] = ld16c(c.rw, zm + 17408);
            if (__all(full4(zf[0][8]) & full4(zf[0][9]) & full4(zf[1][8]) & full4(zf[1][9]))) break;
            if (++spins > SPIN_CAP) { c.dead = true; break; }
        }
        for (;;) {
            #pragma unroll
            for (int i = 0; i < 4; ++i) p[i] = ld16c(c.rw, o0 + i * 1024);
            m0 = ld8c(c.rw, o1); m1 = ld8c(c.rw, o1 + 128);
            if (__all(full4(p[0]) & full4(p[1]) & full4(p[2]) & full4(p[3]) & full2(m0) & full2(m1))) break;
            if (++spins > SPIN_CAP) { c.dead = true; break; }
        }
        f4 a0 = {}, a1 = {};
        #pragma unroll
        for (int i = 0; i < 4; ++i) { a0 = smf(p[i], cat8(zf[0][2 * i], zf[0][2 * i + 1]), a0, sidx); a1 = smf(p[i], cat8(zf[1][2 * i], zf[1][2 * i + 1]), a1, sidx); }
        const u4 pm = {m0.x, m0.y, m1.x, m1.y};
        a0 = smf(pm, cat8(zf[0][8], zf[0][9]), a0, sidx); a1 = smf(pm, cat8(zf[1][8], zf[1][9]), a1, sidx);
        char* w = RM + (s & 1) * 4096 + wave * 512 + ((g & 1) * 4 + (lane >> 5) * 2) * 64 + cc * 4;
        *(LAS float*)(w) = fold32(a0[0], a0[2]); *(LAS float*)(w + 64) = fold32(a0[1], a0[3]);
        *(LAS float*)(w + 2048) = fold32(a1[0], a1[2]); *(LAS float*)(w + 2112) = fold32(a1[1], a1[3]);
        bar();
        if (wave < 2) {
            const char* rp = RM + (s & 1) * 4096 + wave * 2048 + ((lane >> 2) & 7) * 64 + (lane >> 5) * 32 + (lane & 3) * 8;
            const f2 v = (*(const LAS f2*)(rp) + *(const LAS f2*)(rp + 512)) + (*(const LAS f2*)(rp + 1024) + *(const LAS f2*)(rp + 1536));
            st4c(c.rw, om, sane(pk(v.x, v.y)));
        }
        const u4 ones = {~0u, ~0u, ~0u, ~0u};
        st16c(c.rw, zm, ones); st16c(c.rw, zm + 1024, ones); st16c(c.rw, zm + 16384, ones); st16c(c.rw, zm + 17408, ones);
        if (s + 2 < NSEG) {
            if (!__all(zflag >= (unsigned)(s + 2))) wait_flags(c, fzx, s + 2);
            #pragma unroll
            for (int i = 0; i < 8; ++i) { zn[0][i] = ld16c(c.rw, zb0 + (s + 1) * Z_SLOT + i * 1024); zn[1][i] = ld16c(c.rw, zb0 + (s + 1) * Z_SLOT + 32768 + i * 1024); }
            zflag = ld4c(c.rw, fzx);
        }
    }
}

DEV void role_qx(Ctx& c, int o, const unsigned short* __restrict__ Wq) {
    const int lane = c.lane, wave = c.wave, cc = lane & 15, g = lane >> 4;
    char* const QX = c.sm + 73728;
    u8v q0[9], q1[9], k0[9], k1[9];
    {
        u4 r[36];
        fetch36(c, proj_src(c, Wq + 2 * o * DH), 16 * 4096, r);
        load_proj(c, r, proj_src(c, Wq + (2 * o + 1) * DH), 16 * 4096, q0, q1);
        load_proj(c, r, nullptr, 0, k0, k1);
    }
    const int xw = (wave * 16 + cc) * 4 + g * 4 * 272;
    const unsigned fq = OFF_MEM + nearmap(FL_QX + (o * 4 + wave) * 4, c.par0);
    bar();
    xn_dma(c, 0, 0);
    drain();
    bar();
    for (int s = 0; s < NSEG; ++s) {
        const char* XNB = c.sm + (s & 1) * 36864;
        if (s + 1 < NSEG) xn_dma(c, s + 1, ((s + 1) & 1) * 36864);
        f4 acc[2][4] = {};
        #pragma unroll
        for (int i = 0; i < 4; ++i) pre2(acc[0][i], acc[1][i]);
        u4 n0 = lds16(XNB + lane * 16), n1 = lds16(XNB + 18 * 1024 + lane * 16);
        #pragma unroll
        for (int ks = 0; ks < 18; ++ks) {
            const u4 a0 = n0, a1 = n1;
            if (ks < 17) { n0 = lds16(XNB + (ks + 1) * 1024 + lane * 16); n1 = lds16(XNB + (19 + ks) * 1024 + lane * 16); }
            const int t = ks >> 1;
            const u4 b0 = (ks & 1) ? hi4(q0[t]) : lo4(q0[t]), b1 = (ks & 1) ? hi4(q1[t]) : lo4(q1[t]);
            const u4 b2 = (ks & 1) ? hi4(k0[t]) : lo4(k0[t]), b3 = (ks & 1) ? hi4(k1[t]) : lo4(k1[t]);
            mfA(acc[0][0], a0, b0); mfA(acc[1][0], a1, b0); mfA(acc[0][1], a0, b1); mfA(acc[1][1], a1, b1);
            if (t < 7) { mfA(acc[0][2], a0, b2); mfA(acc[1][2], a1, b2); mfA(acc[0][3], a0, b3); mfA(acc[1][3], a1, b3); }
            else { mfV(acc[0][2], a0, b2); mfV(acc[1][2], a1, b2); mfV(acc[0][3], a0, b3); mfV(acc[1][3], a1, b3); }
        }
        drain();
        if (s > 0 && lane == 0) st4c(c.rw, fq, s);
        bar();
        post2(acc[0][3], acc[1][3]);
        #pragma unroll
        for (int i = 0; i < 3; ++i) pin2(acc[0][i], acc[1][i]);
        #pragma unroll
        for (int rt = 0; rt < 2; ++rt)
            #pragma unroll
            for (int r = 0; r < 4; ++r) {
                *(LAS unsigned*)(QX + xw + (rt * 16 + r) * 272) = pk(acc[rt][0][r], acc[rt][1][r]);
                *(LAS unsigned*)(QX + 8704 + xw + (rt * 16 + r) * 272) = pk(acc[rt][2][r], acc[rt][3][r]);
            }
        bar();
        #pragma unroll
        for (int ks = 0; ks < 4; ++ks)
            st16c(c.rw, OFF_QX + ((s * 32 + 2 * o) * 2 + wave) * 4096 + ks * 1024 + lane * 16, lds16(QX + (wave >> 1) * 8704 + ((wave & 1) * 16 + cc) * 272 + ks * 64 + g * 16));
        if (s == 0 || s == NSEG - 1) { drain(); if (lane == 0) st4c(c.rw, fq, s + 1); }
    }
}

DEV void role_out(Ctx& c, int o, const unsigned short* __restrict__ x, unsigned short* __restrict__ out) {
    const int lane = c.lane, wave = c.wave, cc = lane & 15, g = lane >> 4, tid = wave * 64 + lane;
    const int j0 = 3 * (o >> 1);
    const unsigned fl = (g == 0) ? FL_PX + (wave * 16 + cc) * 4 : ((g == 1 ? FL_ZX : FL_ZM) + ((j0 / 9) * 32 + wave * 8 + (cc & 7)) * 4);
    const unsigned flo = OFF_MEM + nearmap(fl, c.par0);
    const bool two = tid < 128;
    const int e0 = (tid / 12) * DM + 16 * j0 + (tid % 12) * 4, e1 = ((two ? tid + 256 : tid) / 12) * DM + 16 * j0 + ((two ? tid + 256 : tid) % 12) * 4;
    const int l0 = (tid / 12) * 208 + (tid % 12) * 16, l1 = ((two ? tid + 256 : tid) / 12) * 208 + ((two ? tid + 256 : tid) % 12) * 16;
    for (int s = o & 1; s < NSEG; s += 2) {
        const bool fin = s * 32 >= P0_ROWS;
        const u2 xv[2] = {*(const u2*)(x + s * 32 * DM + e0), *(const u2*)(x + s * 32 * DM + e1)};
        const unsigned abo[2] = {OFF_AB + (s * 32 * DM + e0) * 2, OFF_AB + (s * 32 * DM + e1) * 2};
        const unsigned zb = OFF_Z + s * Z_SLOT + j0 * 32768 + wave * 8192 + lane * 16;
        wait_flags(c, flo, s + 1);
        const unsigned xb = OFF_PX + s * PX_SLOT + wave * 8 * 2560;
        const unsigned zh = OFF_ZH + s * ZM_SLOT + j0 * 8192 + (wave * 8 + g) * 256 + cc * 16;
        u4 pa[20], zf[3][10];
        #pragma unroll
        for (int i = 0; i < 8; ++i) {
            pa[i] = ld16c(c.rw, xb + i * 2560 + lane * 16); pa[10 + i] = ld16c(c.rw, xb + i * 2560 + 1280 + lane * 16);
            #pragma unroll
            for (int t = 0; t < 3; ++t) zf[t][i] = ld16c(c.rw, zb + t * 32768 + i * 1024);
        }
        #pragma unroll
        for (int i = 0; i < 2; ++i) {
            pa[8 + i] = ld16c(c.rw, xb + (i * 4 + g) * 2560 + 1024 + cc * 16); pa[18 + i] = ld16c(c.rw, xb + (i * 4 + g) * 2560 + 1280 + 1024 + cc * 16);
            #pragma unroll
            for (int t = 0; t < 3; ++t) zf[t][8 + i] = ld16c(c.rw, zh + t * 8192 + i * 1024);
        }
        u2 bv[2] = {};
        if (fin) { bv[0] = ld8c(c.rw, abo[0]); bv[1] = ld8c(c.rw, abo[1]); }
        __builtin_amdgcn_sched_barrier(0);
        f4 acc[2][3] = {};
        #pragma unroll
        for (int i = 0; i < 10; ++i)
            #pragma unroll
            for (int qt = 0; qt < 2; ++qt)
                #pragma unroll
                for (int t = 0; t < 3; ++t) acc[qt][t] = mfma(pa[qt * 10 + i], zf[t][i], acc[qt][t]);
        if (fin && !__all(full2(bv[0]) & full2(bv[1]))) {
            unsigned spins = c.dead ? 8 * SPIN_CAP : 0;
            for (;;) {
                bv[0] = ld8c(c.rw, abo[0]); bv[1] = ld8c(c.rw, abo[1]);
                if (__all(full2(bv[0]) & full2(bv[1]))) break;
                __builtin_amdgcn_s_sleep(8);
                if (++spins > 8 * SPIN_CAP) { c.dead = true; break; }
            }
        }
        char* const R = c.sm + ((s >> 1) & 1) * 26624;
        #pragma unroll
        for (int qt = 0; qt < 2; ++qt)
            #pragma unroll
            for (int r = 0; r < 4; ++r)
                #pragma unroll
                for (int t = 0; t < 3; ++t) *(LAS float*)(R + wave * 6656 + (qt * 16 + g * 4 + r) * 208 + t * 64 + cc * 4) = acc[qt][t][r];
        bar();
        #pragma unroll
        for (int k = 0; k < 2; ++k) {
            if (k == 1 && !two) break;
            const char* rp = R + (k ? l1 : l0);
            const f4 v = (*(const LAS f4*)(rp) + *(const LAS f4*)(rp + 6656)) + (*(const LAS f4*)(rp + 13312) + *(const LAS f4*)(rp + 19968));
            const unsigned a0 = pk(v[0], v[1]), a1 = pk(v[2], v[3]);
            if (fin) {
                const unsigned t0 = pk(bflo(xv[k].x) + bflo(a0), bfhi(xv[k].x) + bfhi(a0)), t1 = pk(bflo(xv[k].y) + bflo(a1), bfhi(xv[k].y) + bfhi(a1));
                *(u2*)(out + s * 32 * DM + (k ? e1 : e0)) = u2{pk(bflo(t0) + bflo(bv[k].x), bfhi(t0) + bfhi(bv[k].x)), pk(bflo(t1) + bflo(bv[k].y), bfhi(t1) + bfhi(bv[k].y))};
            } else st8c(c.rw, abo[k], u2{sane(a0), sane(a1)});
        }
    }
}
DEV float gelu_t(float v) {
    const float u = 0.7978845608028654f * (v + 0.044715f * v * v * v);
    return v * __builtin_amdgcn_rcpf(1.f + __builtin_amdgcn_exp2f(-2.8853900817779268f * u));
}
DEV void mfC(f4& c, u4 a, u4 b) { asm volatile("v_mfma_f32_16x16x32_bf16 %0, %1, %2, %0" : "+a"(c) : "v"(a), "v"(b)); }
DEV void role_ffn(Ctx& c, int f, const unsigned short* __restrict__ x, const unsigned short* __restrict__ Wg, const unsigned short* __restrict__ Wu,
                  const unsigned short* __restrict__ Wd, unsigned short* __restrict__ out) {
    const int lane = c.lane, wave = c.wave, cc = lane & 15, g = lane >> 4, tid = wave * 64 + lane;
    const int p = f & 15, u = f >> 4;
    {
        const u4* src = (const u4*)(c.ws + OFF_XN + p * 147456) + tid;
        u4 r[36];
        #pragma unroll
        for (int i = 0; i < 36; ++i) r[i] = src[i * 256];
        __builtin_amdgcn_sched_barrier(0);
        #pragma unroll
        for (int i = 0; i < 36; ++i) *(LAS u4*)(c.sm + i * 4096 + tid * 16) = r[i];
    }
    bar();
    const unsigned hpark = OFF_HP + (unsigned)f * 196608 + tid * 8;
    const rsrc_t rg_ = __builtin_amdgcn_make_buffer_rsrc((void*)Wg, 0, DM * FF * 2, 0x00020000);
    const rsrc_t ru_ = __builtin_amdgcn_make_buffer_rsrc((void*)Wu, 0, DM * FF * 2, 0x00020000);
    const rsrc_t rd_ = __builtin_amdgcn_make_buffer_rsrc((void*)Wd, 0, DM * FF * 2, 0x00020000);
    #pragma unroll 1
    for (int ct = 0; ct < 3; ++ct) {
        const unsigned wo = (768 * u + 256 * ct + 64 * wave + 4 * cc + (16 * (g >> 1) + 4 * (g & 1)) * FF) * 2;
        f4 ag[8][4] = {}, au[8][4] = {};
        u2 rg[2][8], ru[2][8];
        #pragma unroll
        for (int z = 0; z < 2; ++z)
            #pragma unroll
            for (int e = 0; e < 8; ++e) {
                rg[z][e] = __builtin_amdgcn_raw_buffer_load_b64(rg_, wo, (32 * z + (e & 3) + 8 * (e >> 2)) * FF * 2, 0);
                ru[z][e] = __builtin_amdgcn_raw_buffer_load_b64(ru_, wo, (32 * z + (e & 3) + 8 * (e >> 2)) * FF * 2, 0);
            }
        #pragma unroll 1
        for (int k2 = 0; k2 < 18; k2 += 2) {
            #pragma unroll
            for (int z = 0; z < 2; ++z) {
                const int ks = k2 + z;
                u4 bg[4], bu[4];
                #pragma unroll
                for (int d = 0; d < 4; ++d) {
                    const int ia = (d & 1) * 4 + (d >> 1) * 2;
                    bg[0][d] = perm_lo(rg[z][ia].x, rg[z][ia + 1].x); bg[1][d] = perm_hi(rg[z][ia].x, rg[z][ia + 1].x);
                    bg[2][d] = perm_lo(rg[z][ia].y, rg[z][ia + 1].y); bg[3][d] = perm_hi(rg[z][ia].y, rg[z][ia + 1].y);
                    bu[0][d] = perm_lo(ru[z][ia].x, ru[z][ia + 1].x); bu[1][d] = perm_hi(ru[z][ia].x, ru[z][ia + 1].x);
                    bu[2][d] = perm_lo(ru[z][ia].y, ru[z][ia + 1].y); bu[3][d] = perm_hi(ru[z][ia].y, ru[z][ia + 1].y);
                }
                u4 a[8];
                #pragma unroll
                for (int rt = 0; rt < 8; ++rt) a[rt] = lds16(c.sm + (rt * 18 + ks) * 1024 + lane * 16);
                __builtin_amdgcn_sched_barrier(0);
                const int kn = ks < 16 ? ks + 2 : ks;
                #pragma unroll
                for (int e = 0; e < 8; ++e) {
                    rg[z][e] = __builtin_amdgcn_raw_buffer_load_b64(rg_, wo, (32 * kn + (e & 3) + 8 * (e >> 2)) * FF * 2, 0);
                    ru[z][e] = __builtin_amdgcn_raw_buffer_load_b64(ru_, wo, (32 * kn + (e & 3) + 8 * (e >> 2)) * FF * 2, 0);
                }
                __builtin_amdgcn_sched_barrier(0);
                #pragma unroll
                for (int rt = 0; rt < 8; ++rt)
                    #pragma unroll
                    for (int i = 0; i < 4; ++i) { mfC(ag[rt][i], a[rt], bg[i]); mfC(au[rt][i], a[rt], bu[i]); }
                __builtin_amdgcn_sched_barrier(0);
            }
        }
        asm volatile("s_nop 7\n\ts_nop 2" ::: "memory");
        #pragma unroll
        for (int rt = 0; rt < 8; ++rt)
            #pragma unroll
            for (int r = 0; r < 4; ++r) {
                float hv[4];
                #pragma unroll
                for (int i = 0; i < 4; ++i) {
                    if (r == 0) asm volatile("" : "+a"(ag[rt][i]), "+a"(au[rt][i]));
                    hv[i] = rbf(gelu_t(rbf(ag[rt][i][r]))) * rbf(au[rt][i][r]);
                }
                __builtin_amdgcn_raw_buffer_store_b64(u2{pk(hv[0], hv[1]), pk(hv[2], hv[3])}, c.rw, hpark, ct * 65536 + (rt * 4 + r) * 2048, 0);
                if (r == 3) __builtin_amdgcn_sched_barrier(0);
            }
    }
    drain();
    {
        const unsigned wo = ((768 * u + 8 * g) * DM + 144 * wave + 8 * cc) * 2, wo9 = ((768 * u + 8 * g) * DM + 144 * wave + 128 + cc) * 2;
        f4 acc[8][8] = {}, a9[8] = {};
        u4 rd[2][8]; unsigned rs[2][8];
        #pragma unroll
        for (int z = 0; z < 2; ++z)
            #pragma unroll
            for (int e = 0; e < 8; ++e) {
                rd[z][e] = __builtin_amdgcn_raw_buffer_load_b128(rd_, wo, (32 * z + e) * DM * 2, 0);
                rs[z][e] = __builtin_amdgcn_raw_buffer_load_b16(rd_, wo9, (32 * z + e) * DM * 2, 0);
            }
        #pragma unroll 1
        for (int k2 = 0; k2 < 24; k2 += 2) {
            if ((k2 & 7) == 0) {
                u2 hr[32];
                #pragma unroll
                for (int i = 0; i < 32; ++i) hr[i] = __builtin_amdgcn_raw_buffer_load_b64(c.rw, hpark, (k2 >> 3) * 65536 + i * 2048, 0);
                bar();
                #pragma unroll
                for (int i = 0; i < 32; ++i) *(LAS u2*)(c.sm + ((i >> 2) * 16 + g * 4 + (i & 3)) * 528 + (64 * wave + 4 * cc) * 2) = hr[i];
                bar();
            }
            #pragma unroll
            for (int z = 0; z < 2; ++z) {
                const int ks = k2 + z;
                u4 b[9];
                #pragma unroll
                for (int d = 0; d < 4; ++d) {
                    #pragma unroll
                    for (int q = 0; q < 4; ++q) { b[2 * q][d] = perm_lo(rd[z][2 * d][q], rd[z][2 * d + 1][q]); b[2 * q + 1][d] = perm_hi(rd[z][2 * d][q], rd[z][2 * d + 1][q]); }
                    b[8][d] = perm_lo(rs[z][2 * d], rs[z][2 * d + 1]);
                }
                u4 a[8];
                #pragma unroll
                for (int rt = 0; rt < 8; ++rt) a[rt] = lds16(c.sm + (rt * 16 + cc) * 528 + (ks & 7) * 64 + g * 16);
                __builtin_amdgcn_sched_barrier(0);
                const int kn = ks < 22 ? ks + 2 : ks;
                #pragma unroll
                for (int e = 0; e < 8; ++e) {
                    rd[z][e] = __builtin_amdgcn_raw_buffer_load_b128(rd_, wo, (32 * kn + e) * DM * 2, 0);
                    rs[z][e] = __builtin_amdgcn_raw_buffer_load_b16(rd_, wo9, (32 * kn + e) * DM * 2, 0);
                }
                __builtin_amdgcn_sched_barrier(0);
                #pragma unroll
                for (int rt = 0; rt < 8; ++rt) {
                    #pragma unroll
                    for (int i = 0; i < 8; ++i) mfC(acc[rt][i], a[rt], b[i]);
                    mfV(a9[rt], a[rt], b[8]);
                }
                __builtin_amdgcn_sched_barrier(0);
            }
        }
        asm volatile("s_nop 7\n\ts_nop 2" ::: "memory");
        const unsigned pb = OFF_PART + (unsigned)(p * 6 + u) * (128 * DM * 4) + (g * 4 * DM + 144 * wave) * 4;
        #pragma unroll
        for (int rt = 0; rt < 8; ++rt) {
            #pragma unroll
            for (int i = 0; i < 8; ++i) asm volatile("" : "+a"(acc[rt][i]));
            asm volatile("" : "+v"(a9[rt]));
            #pragma unroll
            for (int r = 0; r < 4; ++r) {
                const unsigned o = pb + (rt * 16 + r) * DM * 4;
                st16c(c.rw, o + cc * 32, __builtin_bit_cast(u4, f4{acc[rt][0][r], acc[rt][1][r], acc[rt][2][r], acc[rt][3][r]}));
                st16c(c.rw, o + cc * 32 + 16, __builtin_bit_cast(u4, f4{acc[rt][4][r], acc[rt][5][r], acc[rt][6][r], acc[rt][7][r]}));
                st4c(c.rw, o + 512 + cc * 4, __float_as_uint(a9[rt][r]));
            }
            __builtin_amdgcn_sched_barrier(0);
        }
    }
    drain();
    bar();
    GAS unsigned* cnt = (GAS unsigned*)(c.ws + OFF_CTL + 1024 + p * 128);
    if (tid == 0) __hip_atomic_fetch_add(cnt, 1u, __ATOMIC_RELAXED, __HIP_MEMORY_SCOPE_AGENT);
    if (wave == 0) {
        unsigned spins = 0;
        while (__hip_atomic_load(cnt, __ATOMIC_RELAXED, __HIP_MEMORY_SCOPE_AGENT) < 6u) { __builtin_amdgcn_s_sleep(8); if (++spins > 4 * SPIN_CAP) break; }
    }
    bar();
    #pragma unroll 1
    for (int it = 0; it < 12; ++it) {
        const int ci = it * 256 + tid, row = ci / 24, col = 96 * u + (ci % 24) * 4;
        const unsigned o = OFF_PART + (unsigned)(p * 6) * (128 * DM * 4) + (row * DM + col) * 4;
        u4 v[6];
        #pragma unroll
        for (int k = 0; k < 6; ++k) v[k] = ld16c(c.rw, o + k * (128 * DM * 4));
        f4 sum = __builtin_bit_cast(f4, v[0]);
        #pragma unroll
        for (int k = 1; k < 6; ++k) sum += __builtin_bit_cast(f4, v[k]);
        const u2 bv = {pk(sum[0], sum[1]), pk(sum[2], sum[3])};
        const int grow = p * 128 + row;
        const unsigned abo = OFF_AB + (grow * DM + col) * 2;
        if (grow >= P0_ROWS) st8c(c.rw, abo, u2{sane(bv.x), sane(bv.y)});
        else {
            const u2 xv = *(const u2*)(x + grow * DM + col);
            u2 av; unsigned spins = c.dead ? 8 * SPIN_CAP : 0;
            for (;;) { av = ld8c(c.rw, abo); if (__all(full2(av))) break; __builtin_amdgcn_s_sleep(32); if (++spins > 8 * SPIN_CAP) { c.dead = true; break; } }
            const unsigned t0 = pk(bflo(xv.x) + bflo(av.x), bfhi(xv.x) + bfhi(av.x)), t1 = pk(bflo(xv.y) + bflo(av.y), bfhi(xv.y) + bfhi(av.y));
            *(u2*)(out + grow * DM + col) = u2{pk(bflo(t0) + bflo(bv.x), bfhi(t0) + bfhi(bv.x)), pk(bflo(t1) + bflo(bv.y), bfhi(t1) + bfhi(bv.y))};
        }
    }
}

__global__ __launch_bounds__(256, 1) void k_main(const unsigned short* __restrict__ x, const unsigned short* __restrict__ Wq, const unsigned short* __restrict__ Wk,
                                                 const unsigned short* __restrict__ Wv, const unsigned short* __restrict__ Wo, const unsigned short* __restrict__ Wg,
                                                 const unsigned short* __restrict__ Wu, const unsigned short* __restrict__ Wd, unsigned short* __restrict__ out, char* ws) {
    extern __shared__ __attribute__((aligned(16))) char smem[];
    Ctx c;
    c.rw = __builtin_amdgcn_make_buffer_rsrc(ws, 0, WS_SPAN, 0x00020000);
    c.ws = ws; c.sm = smem; c.lane = threadIdx.x & 63; c.wave = __builtin_amdgcn_readfirstlane(threadIdx.x >> 6); c.dead = false;
    const unsigned* pw = (const unsigned*)(ws + OFF_CTL);
    c.par0 = __builtin_amdgcn_readfirstlane(pw[0]) & 1; c.par1 = __builtin_amdgcn_readfirstlane(pw[1]) & 1; c.par2 = __builtin_amdgcn_readfirstlane(pw[2]) & 1;
    const int b = blockIdx.x, r8 = b & 7, idx = (b >> 3) * 4 + (r8 & 3);
    if (r8 < 4 && idx < N_CHAIN) {
        if (idx < N_QK) role_qk(c, idx, Wq, Wk);
        else if (idx < N_QK + N_VM) role_vm(c, idx - N_QK, Wv, Wo);
        else role_red(c, idx - N_QK - N_VM);
    } else if (r8 >= 4 && idx < N_OUT + N_ZX + N_QX) {
        if (idx < N_ZX) role_zx(c, idx, Wv, Wo);
        else if (idx < N_ZX + N_QX) role_qx(c, idx - N_ZX, Wq);
        else role_out(c, idx - N_ZX - N_QX, x, out);
    } else {
        const int f = r8 >= 4 ? idx - (N_OUT + N_ZX + N_QX) : idx - N_CHAIN + 56;
        if (f < N_FFN) role_ffn(c, f, x, Wg, Wu, Wd, out);
    }
}

extern "C" void kernel_launch(void* const* d_in, const int* in_sizes, int n_in, void* d_out, int out_size, void* d_ws, size_t ws_size, hipStream_t stream) {
    const unsigned short* x = (const unsigned short*)d_in[0];
    char* ws = (char*)(((uintptr_t)d_ws + MB2 - 1) & ~(uintptr_t)(MB2 - 1));
    (void)hipFuncSetAttribute((const void*)k_main, hipFuncAttributeMaxDynamicSharedMemorySize, LDS_BYTES);
    k_pro<<<257, 256, 0, stream>>>(x, (const unsigned short*)d_in[1], ws);
    k_main<<<256, 256, LDS_BYTES, stream>>>(x, (const unsigned short*)d_in[2], (const unsigned short*)d_in[3], (const unsigned short*)d_in[4],
                                            (const unsigned short*)d_in[5], (const unsigned short*)d_in[8], (const unsigned short*)d_in[9],
                                            (const unsigned short*)d_in[10], (unsigned short*)d_out, ws);
}
```
